```python
import torch

def _softmax(x, axis=-1):
    return torch.softmax(x, dim=axis)
def _gelu(x):
    return torch.nn.functional.gelu(x, approximate="tanh")
def _silu(x):
    return torch.nn.functional.silu(x)
def _sigmoid(x):
    return torch.sigmoid(x)
def _layernorm(x, axis=-1, eps=1e-5):
    m = x.mean(dim=axis, keepdim=True)
    v = x.var(dim=axis, keepdim=True, unbiased=False)
    return (x - m) / torch.sqrt(v + eps)
def _scatter_add(tgt, idx, src, axis=0):
    return tgt.clone().index_add_(axis, idx, src)
def _segment_sum(src, seg, n):
    out = torch.zeros((n,) + src.shape[1:], dtype=src.dtype)
    return out.index_add_(0, seg, src)
def _pad(x, pw):
    flat = tuple(v for pair in reversed(pw) for v in pair)
    return torch.nn.functional.pad(x, flat)

kernel_name = 'gated_mix__compressed_kv_cla__linear_attn_block__prompt'

def setup_inputs():

    _w = (lambda a: (lambda t: t.to(torch.bfloat16) if t.is_floating_point() else t)(torch.as_tensor(a)))
    def _A_setup():
        B, H, Sq, Sk, D, M = 1, 16, 2048, 2048, 256, 448
        Dc = 4 * D
        return {
            "x": _w((lambda *s: torch.randn(*s, dtype=torch.bfloat16) * 0.05)(B, Sq, M)),
            "c_kv": _w((lambda *s: torch.randn(*s, dtype=torch.bfloat16) * 1.6)(B, Sk, Dc)),
            "w_q": _w((lambda *s: torch.randn(*s, dtype=torch.bfloat16) * 3.2)(M, H, D) * (M ** -0.5)),
            "w_uk": _w((lambda *s: torch.randn(*s, dtype=torch.bfloat16) * 3.2)(Dc, H, D) * (Dc ** -0.5)),
            "w_uv": _w((lambda *s: torch.randn(*s, dtype=torch.bfloat16) * 1.6)(Dc, H, D) * (Dc ** -0.5)),
            "w_o": _w((lambda *s: torch.randn(*s, dtype=torch.bfloat16) * 1.6)(H, D, M) * ((H * D) ** -0.5)),
        }
    _A_in = _A_setup()

    def _B_setup():
        B, S, D = 1, 2048, 448
        return {
            "x": _w((lambda *s: torch.randn(*s, dtype=torch.bfloat16) * 0.05)(B, S, D)),
            "wqkv": _w((lambda *s: torch.randn(*s, dtype=torch.bfloat16) * 1.6)(D, 3 * D)),
            "wo": _w((lambda *s: torch.randn(*s, dtype=torch.bfloat16) * 1.6)(D, D)),
        }
    _B_in = _B_setup()

    return {
        "A_x": _A_in["x"],
        "A_c_kv": _A_in["c_kv"],
        "A_w_q": _A_in["w_q"],
        "A_w_uk": _A_in["w_uk"],
        "A_w_uv": _A_in["w_uv"],
        "A_w_o": _A_in["w_o"],
        "B_x": _B_in["x"],
        "B_wqkv": _B_in["wqkv"],
        "B_wo": _B_in["wo"],
        "gate_w": _w((lambda *s: torch.randn(*s, dtype=torch.bfloat16) * 0.05)(448) * 16.0),
    }

def reference(A_x, A_c_kv, A_w_q, A_w_uk, A_w_uv, A_w_o, B_x, B_wqkv, B_wo, gate_w):
    def _A_ref(x, c_kv, w_q, w_uk, w_uv, w_o):
        B, Sq, M = x.shape[0], x.shape[1], x.shape[2]
        Sk = c_kv.shape[1]
        H, D = w_q.shape[1], w_q.shape[2]
        q = torch.einsum("bqm,mhd->bhqd", x, w_q)
        k = torch.einsum("bkc,chd->bhkd", c_kv, w_uk)
        v = torch.einsum("bkc,chd->bhkd", c_kv, w_uv)
        scores = torch.einsum("bhqd,bhkd->bhqk", q, k) * (D ** -0.5)
        off = Sk - Sq
        qi = torch.arange(Sq)[:, None]
        kj = torch.arange(Sk)[None, :]
        cmask = kj <= (qi + off)
        scores = torch.where(cmask[None, None], scores, -1e9)
        p = _softmax(scores, axis=-1)
        ctx = torch.einsum("bhqk,bhkd->bhqd", p, v)
        return torch.einsum("bhqd,hdm->bqm", ctx, w_o)

    def _B_ref(x, wqkv, wo):
        B, S, D = x.shape
        H = 16; Dh = D // H
        qkv = (lambda x, *d: x.permute(*d))((x @ wqkv).reshape(B, S, 3, H, Dh), 2, 0, 3, 1, 4)
        q, k, v = qkv[0], qkv[1], qkv[2]
        phi = lambda t: torch.where(t > 0, t + 1, torch.exp(t)) + 1e-6
        pq, pk = phi(q), phi(k)
        kv = torch.einsum("bhsd,bhse->bhde", pk, v)
        z = torch.einsum("bhsd->bhd", pk)
        num = torch.einsum("bhqd,bhde->bhqe", pq, kv)
        den = torch.einsum("bhqd,bhd->bhq", pq, z)[..., None] + 1e-6
        o = num / den
        return (lambda x, *d: x.permute(*d))(o, 0, 2, 1, 3).reshape(B, S, D) @ wo

    x = A_x
    g = _sigmoid((x * gate_w).sum(-1, keepdims=True))
    a = _A_ref(x, A_c_kv, A_w_q, A_w_uk, A_w_uv, A_w_o)
    b = _B_ref(x, B_wqkv, B_wo)
    return g * a + (1 - g) * b
```

```cpp
#include <hip/hip_runtime.h>
#include <hip/hip_bf16.h>
typedef unsigned short u16;
__device__ __forceinline__ float bf2f(u16 b){ return __uint_as_float(((unsigned)b)<<16); }
__device__ __forceinline__ u16 f2bf(float f){ unsigned u=__float_as_uint(f); u += 0x7FFFu + ((u>>16)&1u); return (u16)(u>>16); }
__device__ __forceinline__ float rbf(float f){ return bf2f(f2bf(f)); }

__global__ void gemm_naive(const u16* A, const u16* B, u16* C, int M, int N, int K){
  __shared__ float As[16][17], Bs[16][17];
  int tx=threadIdx.x&15, ty=threadIdx.x>>4;
  int row=blockIdx.y*16+ty, col=blockIdx.x*16+tx;
  float acc=0;
  for(int k0=0;k0<K;k0+=16){
    As[ty][tx]=bf2f(A[(size_t)row*K+k0+tx]);
    Bs[ty][tx]=bf2f(B[(size_t)(k0+ty)*N+col]);
    __syncthreads();
    for(int k=0;k<16;k++) acc+=As[ty][k]*Bs[k][tx];
    __syncthreads();
  }
  C[(size_t)row*N+col]=f2bf(acc);
}
__global__ void attn_naive(const u16* Q, const u16* Kk, const u16* V, u16* ctx){
  __shared__ float s[2048]; __shared__ float q[256]; __shared__ float red[256];
  int h=blockIdx.y, qi=blockIdx.x, t=threadIdx.x;
  q[t]=bf2f(Q[(size_t)qi*4096+h*256+t]);
  __syncthreads();
  float mx=-1e30f;
  for(int k=t;k<=qi;k+=256){ float a=0; const u16* kp=Kk+(size_t)k*4096+h*256; for(int d=0;d<256;d++) a+=q[d]*bf2f(kp[d]); a=a*0.0625f; s[k]=a; mx=fmaxf(mx,a);}
  red[t]=mx; __syncthreads();
  for(int o=128;o>0;o>>=1){ if(t<o) red[t]=fmaxf(red[t],red[t+o]); __syncthreads(); }
  mx=red[0]; __syncthreads();
  float sum=0; for(int k=t;k<=qi;k+=256){ float e=__expf(s[k]-mx); s[k]=e; sum+=e; }
  red[t]=sum; __syncthreads();
  for(int o=128;o>0;o>>=1){ if(t<o) red[t]+=red[t+o]; __syncthreads(); }
  sum=red[0];
  float acc=0; for(int k=0;k<=qi;k++) acc+=rbf(s[k])*bf2f(V[(size_t)k*4096+h*256+t]);
  ctx[(size_t)qi*4096+h*256+t]=f2bf(acc/sum);
}
__device__ __forceinline__ float phi(float t){ float r = t>0? rbf(t+1.f) : rbf(__expf(t)); return rbf(r+1e-6f); }
__global__ void kv_naive(const u16* qkv, float* kv){
  int h=blockIdx.x, t=threadIdx.x; if(t>=28*29) return; int d=t/29, e=t%29;
  float acc=0;
  for(int s=0;s<2048;s++){ float pk=phi(bf2f(qkv[(size_t)s*1344+448+h*28+d])); float v= e<28? bf2f(qkv[(size_t)s*1344+896+h*28+e]) : 1.f; acc+=pk*v; }
  kv[h*28*29+t]=rbf(acc);
}
__global__ void ob_naive(const u16* qkv, const float* kv, u16* o){
  int s=blockIdx.x, t=threadIdx.x; if(t>=448) return; int h=t/28, e=t%28;
  float num=0, den=0;
  for(int d=0;d<28;d++){ float pq=phi(bf2f(qkv[(size_t)s*1344+h*28+d])); num+=pq*kv[h*812+d*29+e]; den+=pq*kv[h*812+d*29+28]; }
  o[(size_t)s*448+t]=f2bf(rbf(num)/rbf(rbf(den)+1e-6f));
}
__global__ void mix(const u16* x, const u16* gw, const u16* a, const u16* b, u16* out){
  __shared__ float red[512];
  int s=blockIdx.x, t=threadIdx.x;
  red[t]= t<448? rbf(bf2f(x[(size_t)s*448+t])*bf2f(gw[t])) : 0.f; __syncthreads();
  for(int o=256;o>0;o>>=1){ if(t<o) red[t]+=red[t+o]; __syncthreads(); }
  float z=rbf(red[0]); float g=rbf(1.f/(1.f+__expf(-z)));
  if(t<448) out[(size_t)s*448+t]=f2bf(rbf(g*bf2f(a[(size_t)s*448+t]))+rbf(rbf(1.f-g)*bf2f(b[(size_t)s*448+t])));
}
extern "C" void kernel_launch(void* const* d_in, const int* in_sizes, int n_in, void* d_out, int out_size, void* d_ws, size_t ws_size, hipStream_t stream){
  const u16* x=(const u16*)d_in[0]; const u16* c=(const u16*)d_in[1]; const u16* wq=(const u16*)d_in[2]; const u16* wuk=(const u16*)d_in[3];
  const u16* wuv=(const u16*)d_in[4]; const u16* wo=(const u16*)d_in[5]; const u16* wqkv=(const u16*)d_in[7]; const u16* bwo=(const u16*)d_in[8]; const u16* gw=(const u16*)d_in[9];
  u16* ws=(u16*)d_ws; size_t T=(size_t)2048*4096;
  u16 *Q=ws, *K=ws+T, *V=ws+2*T, *ctx=ws+3*T, *qkv=ws+4*T, *ob=qkv+2048*1344, *a=ob+2048*448, *b=a+2048*448; float* kv=(float*)(b+2048*448);
  gemm_naive<<<dim3(256,128),256,0,stream>>>(x,wq,Q,2048,4096,448);
  gemm_naive<<<dim3(256,128),256,0,stream>>>(c,wuk,K,2048,4096,1024);
  gemm_naive<<<dim3(256,128),256,0,stream>>>(c,wuv,V,2048,4096,1024);
  gemm_naive<<<dim3(84,128),256,0,stream>>>(x,wqkv,qkv,2048,1344,448);
  attn_naive<<<dim3(2048,16),256,0,stream>>>(Q,K,V,ctx);
  kv_naive<<<16,1024,0,stream>>>(qkv,kv);
  ob_naive<<<2048,512,0,stream>>>(qkv,kv,ob);
  gemm_naive<<<dim3(28,128),256,0,stream>>>(ctx,wo,a,2048,448,4096);
  gemm_naive<<<dim3(28,128),256,0,stream>>>(ob,bwo,b,2048,448,448);
  mix<<<2048,512,0,stream>>>(x,gw,a,b,(u16*)d_out);
}
```

```cpp
#define ACLOB3 "a0","a1","a2","a3","a4","a5","a6","a7","a8","a9","a10","a11","a12","a13","a14","a15","a16","a17","a18","a19","a20","a21","a22","a23","a24","a25","a26","a27","a28","a29","a30","a31","a32","a33","a34","a35","a36","a37","a38","a39","a40","a41","a42","a43","a44","a45","a46","a47","a48","a49","a50","a51","a52","a53","a54","a55","a56","a57","a58","a59","a60","a61","a62","a63","a64","a65","a66","a67","a68","a69","a70","a71","a72","a73","a74","a75","a76","a77","a78","a79","a80","a81","a82","a83","a84","a85","a86","a87","a88","a89","a90","a91","a92","a93","a94","a95"
struct Op {
  const LAS char* wb[2];
  unsigned vC[4], vW[4], m0w;
  rsrc_t srdC, srdW, srdL;
  f32x4 acc[4][4];
  bf16x8 wf[4];
};
template <int ST, bool LIN, int I> DI void op_issue1(const Op& c, unsigned csoff, unsigned wsoff) {
  if constexpr (I < 4) {
    if constexpr (I == 0) asm volatile("s_mov_b32 m0, %0\n\ts_nop 0\n\tbuffer_load_dwordx4 %1, %2, %3 offen lds" :: "s"(c.m0w + ST * 4096), "v"(c.vW[0]), "s"(LIN ? c.srdL : c.srdW), "s"(wsoff) : "memory");
    else asm volatile("buffer_load_dwordx4 %0, %1, %2 offen offset:%3 lds" :: "v"(c.vW[I]), "s"(LIN ? c.srdL : c.srdW), "s"(wsoff), "i"(I * 1024) : "memory");
  } else {
    constexpr int R = ST * 16 + (I - 4) * 4;
    asm volatile("buffer_load_dwordx4 a[%c3:%c4], %0, %1, %2 offen" :: "v"(c.vC[I - 4]), "s"(c.srdC), "s"(csoff), "i"(R), "i"(R + 3) : "memory");
  }
}
template <int ST, bool LIN, int... Is> DI void op_issue(const Op& c, unsigned csoff, unsigned wsoff, iseq<Is...>) { (op_issue1<ST, LIN, Is>(c, csoff, wsoff), ...); }
template <int ST, int U> DI void op_wread(const Op& c, bf16x8 (&nf)[4]) { constexpr int nt = U >> 1, t = U & 1; half_ld(nf[nt], t, c.wb[nt & 1] + ST * 4096 + t * 2048 + (nt >> 1) * 256); }
template <int ST, bool FIRST, bool RDNEXT, int PF, int G> DI void op_gap(Op& c, bf16x8 (&nf)[4], unsigned csoff, unsigned wsoff) {
  constexpr int nt = G >> 2, i = G & 3, R = ST * 16 + i * 4;
  if constexpr (FIRST) asm volatile("v_mfma_f32_16x16x32_bf16 %0, %1, a[%c2:%c3], 0" : "=&v"(c.acc[nt][i]) : "v"(c.wf[nt]), "i"(R), "i"(R + 3));
  else                 asm volatile("v_mfma_f32_16x16x32_bf16 %0, %1, a[%c2:%c3], %0" : "+v"(c.acc[nt][i]) : "v"(c.wf[nt]), "i"(R), "i"(R + 3));
  if constexpr (RDNEXT && G < 8) op_wread<(ST + 1) % 6, G>(c, nf);
  if constexpr (PF != 0 && (G & 1) == 1) op_issue1<(ST + 5) % 6, PF == 2, (G >> 1)>(c, csoff, wsoff);
  SBAR();
}
template <int ST, bool FIRST, bool RDNEXT, int PF, int NV, int... Gs> DI void op_stage_(Op& c, unsigned csoff, unsigned wsoff, iseq<Gs...>) {
  bf16x8 nf[4];
  if constexpr (RDNEXT) asm volatile("s_waitcnt vmcnt(%0)" :: "i"(NV) : ACLOB3, "memory");
  (op_gap<ST, FIRST, RDNEXT, PF, Gs>(c, nf, csoff, wsoff), ...);
  if constexpr (RDNEXT) { c.wf[0] = nf[0]; c.wf[1] = nf[1]; c.wf[2] = nf[2]; c.wf[3] = nf[3]; }
}
template <int ST, bool FIRST, bool RDNEXT, int PF, int NV> DI void op_stage(Op& c, int jpf, unsigned wid) {
  const unsigned kap = 4 * jpf + wid;
  op_stage_<ST, FIRST, RDNEXT, PF, NV>(c, kap * 1024, PF == 2 ? (kap - 128) * 28 * 896 : kap * 32 * 896, make_iseq<16>{});
}

__global__ __launch_bounds__(256, 1) void k3_out(const u16* wo, const u16* bwo, const char* ws, u16* out) {
  extern __shared__ __attribute__((aligned(16))) char smem[];
  const LAS char* lds = (const LAS char*)smem;
  const unsigned lane = threadIdx.x & 63, wid = rfl(threadIdx.x >> 6), r = lane & 15, g = lane >> 4;
  const int b = blockIdx.x, xcd = b & 7, idx = b >> 3, rt = xcd * 4 + (idx & 3), ct = idx >> 2;
  Op c;
  const unsigned lds0 = (unsigned)(uintptr_t)lds;
#pragma unroll
  for (int par = 0; par < 2; ++par) { c.wb[par] = lds + (wid * 24576 + g * 512 + ((lane >> 2) & 3) * 64 + ((par ^ (g & 1)) * 32) + (lane & 3) * 8); PIN_V(c.wb[par]); }
#pragma unroll
  for (int i = 0; i < 4; ++i) { c.vC[i] = lane * 16 + i * CTX_STRIP;
    c.vW[i] = (4 * (lane >> 5) + ((lane >> 2) & 3)) * 896 + ((lane >> 4) & 1) * 64 + ((lane & 3) ^ (2 * (lane >> 5))) * 16 + i * (7168 - 1024); }
  c.m0w = lds0 + wid * 24576;
  c.srdC = mk_rsrc(ws + WS_CTX + (size_t)rt * 4 * CTX_STRIP, 4 * CTX_STRIP);
  c.srdW = mk_rsrc(wo + ct * 64, 4096 * 896 - ct * 128);
  c.srdL = mk_rsrc(bwo + ct * 64, 448 * 896 - ct * 128);
  op_issue<0, false>(c, (0 + wid) * 1024, (0 + wid) * 32 * 896, make_iseq<8>{});
  op_issue<1, false>(c, (4 + wid) * 1024, (4 + wid) * 32 * 896, make_iseq<8>{});
  op_issue<2, false>(c, (8 + wid) * 1024, (8 + wid) * 32 * 896, make_iseq<8>{});
  op_issue<3, false>(c, (12 + wid) * 1024, (12 + wid) * 32 * 896, make_iseq<8>{});
  op_issue<4, false>(c, (16 + wid) * 1024, (16 + wid) * 32 * 896, make_iseq<8>{});
  asm volatile("s_waitcnt vmcnt(32)" ::: ACLOB3, "memory");
  op_wread<0, 0>(c, c.wf); op_wread<0, 1>(c, c.wf); op_wread<0, 2>(c, c.wf); op_wread<0, 3>(c, c.wf);
  op_wread<0, 4>(c, c.wf); op_wread<0, 5>(c, c.wf); op_wread<0, 6>(c, c.wf); op_wread<0, 7>(c, c.wf);
  SBAR();
  op_stage<0, true, true, 1, 24>(c, 5, wid);
  for (int j = 1; j < 25; j += 6) {
    op_stage<1, false, true, 1, 24>(c, j + 5, wid); op_stage<2, false, true, 1, 24>(c, j + 6, wid); op_stage<3, false, true, 1, 24>(c, j + 7, wid);
    op_stage<4, false, true, 1, 24>(c, j + 8, wid); op_stage<5, false, true, 1, 24>(c, j + 9, wid); op_stage<0, false, true, 1, 24>(c, j + 10, wid);
  }
  op_stage<1, false, true, 1, 24>(c, 30, wid); op_stage<2, false, true, 1, 24>(c, 31, wid);
  op_stage<3, false, true, 2, 24>(c, 32, wid); op_stage<4, false, true, 2, 24>(c, 33, wid); op_stage<5, false, true, 2, 24>(c, 34, wid); op_stage<0, false, true, 2, 24>(c, 35, wid);
  op_stage<1, false, true, 0, 24>(c, 0, wid); op_stage<2, false, true, 0, 16>(c, 0, wid); op_stage<3, false, true, 0, 8>(c, 0, wid); op_stage<4, false, true, 0, 0>(c, 0, wid);
  op_stage<5, false, false, 0, 0>(c, 0, wid);
  asm volatile("s_nop 7\n\ts_nop 1" ::: "memory");
#pragma unroll
  for (int nt = 0; nt < 4; ++nt)
#pragma unroll
    for (int i = 0; i < 4; ++i) PIN_V(c.acc[nt][i]);
  LAS char* red = (LAS char*)lds;
#pragma unroll
  for (int nt = 0; nt < 4; ++nt)
#pragma unroll
    for (int i = 0; i < 4; ++i) *(LAS f32x4*)(red + wid * 24576 + (i * 4 + nt) * 1024 + lane * 16) = c.acc[nt][i];
  asm volatile("s_waitcnt lgkmcnt(0)\n\ts_barrier" ::: "memory");
  u16* orow = out + (size_t)(rt * 64 + wid * 16 + r) * 448 + ct * 64 + g * 4;
#pragma unroll
  for (int nt = 0; nt < 4; ++nt) {
    f32x4 s = *(const LAS f32x4*)(red + (wid * 4 + nt) * 1024 + lane * 16);
#pragma unroll
    for (int w = 1; w < 4; ++w) s += *(const LAS f32x4*)(red + w * 24576 + (wid * 4 + nt) * 1024 + lane * 16);
    *(u32x2*)(orow + nt * 16) = u32x2{cvtpk(s[0], s[1]), cvtpk(s[2], s[3])};
  }
}

extern "C" void kernel_launch(void* const* d_in, const int* in_sizes, int n_in, void* d_out, int out_size, void* d_ws, size_t ws_size, hipStream_t stream) {
  (void)hipFuncSetAttribute((const void*)k1_proj, hipFuncAttributeMaxDynamicSharedMemorySize, 163840);
  (void)hipFuncSetAttribute((const void*)k2_attn, hipFuncAttributeMaxDynamicSharedMemorySize, 163840);
  (void)hipFuncSetAttribute((const void*)k3_out, hipFuncAttributeMaxDynamicSharedMemorySize, 98304);
  k1_proj<<<256, 256, 163840, stream>>>((const u16*)d_in[0], (const u16*)d_in[1], (const u16*)d_in[2], (const u16*)d_in[3], (const u16*)d_in[4], (const u16*)d_in[7], (const u16*)d_in[9], (char*)d_ws);
  k2_attn<<<256, 256, 163840, stream>>>((char*)d_ws);
  k3_out<<<224, 256, 98304, stream>>>((const u16*)d_in[5], (const u16*)d_in[8], (const char*)d_ws, (u16*)d_out);
}
```

```cpp
constexpr int OP_STAGE = 28672;
#define ACLOB3 "a0","a1","a2","a3","a4","a5","a6","a7","a8","a9","a10","a11","a12","a13","a14","a15","a16","a17","a18","a19","a20","a21","a22","a23","a24","a25","a26","a27","a28","a29","a30","a31","a32","a33","a34","a35","a36","a37","a38","a39","a40","a41","a42","a43","a44","a45","a46","a47","a48","a49","a50","a51","a52","a53","a54","a55","a56","a57","a58","a59","a60","a61","a62","a63","a64","a65","a66","a67","a68","a69","a70","a71","a72","a73","a74","a75","a76","a77","a78","a79","a80","a81","a82","a83","a84","a85","a86","a87","a88","a89","a90","a91","a92","a93","a94","a95","a96","a97","a98","a99","a100","a101","a102","a103","a104","a105","a106","a107","a108","a109","a110","a111","a112","a113","a114","a115","a116","a117","a118","a119","a120","a121","a122","a123","a124","a125","a126","a127","a128","a129","a130","a131","a132","a133","a134","a135","a136","a137","a138","a139","a140","a141","a142","a143","a144","a145","a146","a147","a148","a149","a150","a151","a152","a153","a154","a155","a156","a157","a158","a159"
struct Op {
  const LAS char* wb[3];
  unsigned vC[2], vW[7], m0w;
  unsigned cs0, csl, ws0, wsl;
  rsrc_t srdC, srdW, srdL;
  f32x4 acc[7][2];
  bf16x8 pre[7];
};
template <int ST, int I> DI void op_load(const Op& c) {
  constexpr int slot = ST % 5;
  if constexpr (I < 7) {
    const unsigned so = ST < 8 ? c.ws0 + ST * 4 * 32 * 896 : c.wsl;
    if constexpr ((I & 3) == 0) asm volatile("s_mov_b32 m0, %0\n\ts_nop 0\n\tbuffer_load_dwordx4 %1, %2, %3 offen lds" :: "s"(c.m0w + slot * OP_STAGE + I * 1024), "v"(c.vW[I]), "s"(ST < 8 ? c.srdW : c.srdL), "s"(so) : "memory");
    else asm volatile("buffer_load_dwordx4 %0, %1, %2 offen offset:%3 lds" :: "v"(c.vW[I]), "s"(ST < 8 ? c.srdW : c.srdL), "s"(so), "i"((I & 3) * 1024) : "memory");
  } else {
    constexpr int ks = (I - 7) >> 1, st = (I - 7) & 1, R = slot * 32 + ks * 8 + st * 4;
    const unsigned so = ST < 8 ? c.cs0 + ST * 4096 : c.csl;
    asm volatile("buffer_load_dwordx4 a[%c3:%c4], %0, %1, %2 offen offset:%5" :: "v"(c.vC[st]), "s"(c.srdC), "s"(so), "i"(R), "i"(R + 3), "i"(ks * 1024) : "memory");
  }
}
template <int ST, int... Is> DI void op_loads(const Op& c, iseq<Is...>) { (op_load<ST, Is>(c), ...); }
template <int ST, int KS, int NT, int T> DI void op_wread(const Op& c, bf16x8& dst) {
  constexpr int off = (ST % 5) * OP_STAGE + KS * 7168 + T * 3584 + NT * 32, hb = off / 57344;
  half_ld(dst, T, c.wb[hb] + (off - hb * 57344));
}
struct OpF { bf16x8 f[35]; };
template <int ST, int NV, int G> DI void op_gap(Op& c, OpF& h) {
  constexpr int fi = G >> 1, ks = fi / 7, nt = fi % 7, st = G & 1, R = (ST % 5) * 32 + ks * 8 + st * 4;
  if constexpr (G == 42 && ST < 8) { LGKM(0); asm volatile("s_waitcnt vmcnt(%0)\n\ts_barrier" :: "i"(NV) : ACLOB3, "memory"); }
  if constexpr (ST == 0 && ks == 0) asm volatile("v_mfma_f32_16x16x32_bf16 %0, %1, a[%c2:%c3], 0" : "=&v"(c.acc[nt][st]) : "v"(h.f[fi]), "i"(R), "i"(R + 3));
  else                              asm volatile("v_mfma_f32_16x16x32_bf16 %0, %1, a[%c2:%c3], %0" : "+v"(c.acc[nt][st]) : "v"(h.f[fi]), "i"(R), "i"(R + 3));
  if constexpr (G < 36) {
    constexpr int u0 = G + G / 6;
    { constexpr int f2 = 7 + (u0 >> 1); op_wread<ST, f2 / 7, f2 % 7, (u0 & 1)>(c, h.f[f2]); }
    if constexpr (G % 6 == 5) { constexpr int u1 = u0 + 1, f2 = 7 + (u1 >> 1); op_wread<ST, f2 / 7, f2 % 7, (u1 & 1)>(c, h.f[f2]); }
  }
  if constexpr (G >= 42 && ST < 8) op_wread<ST + 1, 0, ((G - 42) >> 1), (G & 1)>(c, h.f[28 + ((G - 42) >> 1)]);
  if constexpr (ST + 4 <= 8 && G < 45 && G % 3 == 1) op_load<ST + 4, G / 3>(c);
  SBAR();
}
template <int ST, int NV, int... Gs> DI void op_stage_(Op& c, iseq<Gs...>) {
  OpF h;
#pragma unroll
  for (int i = 0; i < 7; ++i) h.f[i] = c.pre[i];
  (op_gap<ST, NV, Gs>(c, h), ...);
  if constexpr (ST < 8) {
#pragma unroll
    for (int i = 0; i < 7; ++i) c.pre[i] = h.f[28 + i];
  }
}
template <int ST, int NV> DI void op_stage(Op& c) { op_stage_<ST, NV>(c, make_iseq<56>{}); }
template <int... Us> DI void op_first(Op& c, iseq<Us...>) { (op_wread<0, 0, (Us >> 1), (Us & 1)>(c, c.pre[Us >> 1]), ...); }

__global__ __launch_bounds__(256, 1) void k3_out(const u16* wo, const u16* bwo, char* ws, u16* out) {
  extern __shared__ __attribute__((aligned(16))) char smem[];
  const LAS char* lds = (const LAS char*)smem;
  const unsigned lane = threadIdx.x & 63, wid = rfl(threadIdx.x >> 6), r = lane & 15, g = lane >> 4;
  const int b = blockIdx.x, xcd = b & 7, idx = b >> 3;
  const int kq = xcd >> 1, rb = (xcd & 1) * 8 + (idx & 7), nq = idx >> 3;
  Op c;
  const unsigned lds0 = (unsigned)(uintptr_t)lds;
#pragma unroll
  for (int hb = 0; hb < 3; ++hb) { c.wb[hb] = lds + (hb * 57344 + (4 * g + ((lane >> 2) & 3)) * 224 + (lane & 3) * 8); PIN_V(c.wb[hb]); }
  c.vC[0] = lane * 16; c.vC[1] = lane * 16 + CTX_STRIP;
#pragma unroll
  for (int p = 0; p < 7; ++p) { const unsigned ch = 64 * p + lane, row = ch / 14, col = ch - row * 14; c.vW[p] = row * 896 + col * 16 - (p & 3) * 1024; }
  c.m0w = lds0 + wid * 7168;
  c.srdC = mk_rsrc(ws + WS_CTX + (size_t)(rb * 8 + wid * 2) * CTX_STRIP, 2 * CTX_STRIP);
  c.srdW = mk_rsrc(wo + nq * 112, 4096 * 896 - nq * 224);
  c.srdL = mk_rsrc(bwo + nq * 112, 448 * 896 - nq * 224);
  c.cs0 = kq * 32 * 1024; c.csl = (128 + kq * 4) * 1024;
  c.ws0 = (kq * 32 + wid) * 32 * 896; c.wsl = (kq * 4 + wid) * 28 * 896;
  op_loads<0>(c, make_iseq<15>{}); op_loads<1>(c, make_iseq<15>{}); op_loads<2>(c, make_iseq<15>{}); op_loads<3>(c, make_iseq<15>{});
  asm volatile("s_waitcnt vmcnt(45)\n\ts_barrier" ::: ACLOB3, "memory");
  op_first(c, make_iseq<14>{});
  SBAR();
  op_stage<0, 44>(c); op_stage<1, 44>(c); op_stage<2, 44>(c); op_stage<3, 44>(c); op_stage<4, 44>(c);
  op_stage<5, 30>(c); op_stage<6, 15>(c); op_stage<7, 0>(c); op_stage<8, 0>(c);
  asm volatile("s_nop 7\n\ts_nop 1" ::: "memory");
#pragma unroll
  for (int nt = 0; nt < 7; ++nt) { PIN_V(c.acc[nt][0]); PIN_V(c.acc[nt][1]); }
  const int grp = (rb * 4 + nq) * 4 + wid;
  const rsrc_t srdX = mk_rsrc(ws + WS_XCH + (size_t)grp * 4 * 14336, 4 * 14336);
  unsigned* fl = (unsigned*)(ws + WS_FLAG2 + (size_t)grp * 256);
  if ((int)wid != kq) {
#pragma unroll
    for (int nt = 0; nt < 7; ++nt)
#pragma unroll
      for (int st = 0; st < 2; ++st) __builtin_amdgcn_raw_buffer_store_b128(__builtin_bit_cast(u32x4, c.acc[nt][st]), srdX, lane * 16 + (nt * 2 + st) * 1024, kq * 14336, 16);
    asm volatile("s_waitcnt vmcnt(0)" ::: "memory");
    if (lane == 0) __hip_atomic_store((GAS unsigned*)(fl + kq * 16), 1u, __ATOMIC_RELAXED, __HIP_MEMORY_SCOPE_AGENT);
  } else {
    const unsigned src = lane & 3;
    for (unsigned spin = 0; spin < (1u << 22); ++spin) {
      const unsigned v = __hip_atomic_load((GAS unsigned*)(fl + src * 16), __ATOMIC_RELAXED, __HIP_MEMORY_SCOPE_AGENT);
      if (__builtin_amdgcn_ballot_w64(v == 0u && (int)src != kq) == 0ull) break;
      __builtin_amdgcn_s_sleep(1);
    }
    asm volatile("" ::: "memory");
    u32x4 pt[3][14];
#pragma unroll
    for (int j = 0; j < 3; ++j) { const int s = (kq + 1 + j) & 3;
#pragma unroll
      for (int t = 0; t < 14; ++t) pt[j][t] = __builtin_amdgcn_raw_buffer_load_b128(srdX, lane * 16 + t * 1024, s * 14336, 16); }
#pragma unroll
    for (int st = 0; st < 2; ++st) {
      u16* orow = out + (size_t)(rb * 128 + wid * 32 + st * 16 + r) * 448 + nq * 112 + g * 4;
#pragma unroll
      for (int nt = 0; nt < 7; ++nt) {
        const f32x4 s = ((c.acc[nt][st] + __builtin_bit_cast(f32x4, pt[0][nt * 2 + st])) + __builtin_bit_cast(f32x4, pt[1][nt * 2 + st])) + __builtin_bit_cast(f32x4, pt[2][nt * 2 + st]);
        *(u32x2*)(orow + nt * 16) = u32x2{cvtpk(s[0], s[1]), cvtpk(s[2], s[3])};
      }
    }
  }
}

extern "C" void kernel_launch(void* const* d_in, const int* in_sizes, int n_in, void* d_out, int out_size, void* d_ws, size_t ws_size, hipStream_t stream) {
  (void)hipFuncSetAttribute((const void*)k1_proj, hipFuncAttributeMaxDynamicSharedMemorySize, 163840);
  (void)hipFuncSetAttribute((const void*)k2_attn, hipFuncAttributeMaxDynamicSharedMemorySize, 163840);
  (void)hipFuncSetAttribute((const void*)k3_out, hipFuncAttributeMaxDynamicSharedMemorySize, 143360);
  k1_proj<<<256, 256, 163840, stream>>>((const u16*)d_in[0], (const u16*)d_in[1], (const u16*)d_in[2], (const u16*)d_in[3], (const u16*)d_in[4], (const u16*)d_in[7], (const u16*)d_in[9], (char*)d_ws);
  k2_attn<<<256, 256, 163840, stream>>>((char*)d_ws, (const u16*)d_in[5], (const u16*)d_in[8]);
  k3_out<<<256, 256, 143360, stream>>>((const u16*)d_in[5], (const u16*)d_in[8], (char*)d_ws, (u16*)d_out);
}
```

```cpp
#include <hip/hip_runtime.h>
#include <stdint.h>
#include <utility>
template <int... Is> using iseq = std::integer_sequence<int, Is...>;
template <int N> using make_iseq = std::make_integer_sequence<int, N>;
typedef unsigned short u16;
typedef unsigned bf16x8 __attribute__((ext_vector_type(4)));
typedef unsigned s16x4 __attribute__((ext_vector_type(2)));
typedef short i16x4 __attribute__((ext_vector_type(4)));
typedef float f32x4 __attribute__((ext_vector_type(4)));
typedef float f32x2 __attribute__((ext_vector_type(2)));
typedef __bf16 bf16x2_t __attribute__((ext_vector_type(2)));
typedef unsigned u32x4 __attribute__((ext_vector_type(4)));
typedef unsigned u32x2 __attribute__((ext_vector_type(2)));
typedef __amdgpu_buffer_rsrc_t rsrc_t;
#define LAS __attribute__((address_space(3)))
#define GAS __attribute__((address_space(1)))
#define DI __device__ __forceinline__
#define SBAR() __builtin_amdgcn_sched_barrier(0)
#define PIN_V(x) asm volatile("" : "+v"(x))
#define PIN_S(x) asm volatile("" : "+s"(x))
#define LGKM(n) __builtin_amdgcn_s_waitcnt(0xC07F | ((n) << 8))
#define WAIT_BAR(n) do { __builtin_amdgcn_s_waitcnt(0x0070 | ((n) & 15) | (((n) >> 4) << 14)); asm volatile("s_barrier" ::: "memory"); SBAR(); } while (0)

DI unsigned cvtpk(float lo, float hi) { f32x2 v = {lo, hi}; bf16x2_t b = __builtin_convertvector(v, bf16x2_t); return __builtin_bit_cast(unsigned, b); }
DI float rbf(float x) { return __uint_as_float(cvtpk(x, 0.f) << 16); }
DI float bflo(unsigned w) { return __uint_as_float(w << 16); }
DI float bfhi(unsigned w) { return __uint_as_float(w & 0xffff0000u); }
DI unsigned rfl(unsigned x) { return __builtin_amdgcn_readfirstlane(x); }
DI rsrc_t mk_rsrc(const void* p, unsigned bytes) { return __builtin_amdgcn_make_buffer_rsrc((void*)p, (short)0, (int)bytes, 0x00020000); }
DI void dma16(unsigned m0v, unsigned voff, rsrc_t srd, unsigned soff) {
  asm volatile("s_mov_b32 m0, %0\n\ts_nop 0\n\tbuffer_load_dwordx4 %1, %2, %3 offen lds" :: "s"(m0v), "v"(voff), "s"(srd), "s"(soff) : "memory");
}
template <int LOFF, int SOFF, bool SC1 = false> DI void dma16i(unsigned m0base, unsigned voff, rsrc_t srd) {
  unsigned t;
  if constexpr (SC1) asm volatile("s_add_u32 m0, %1, %2\n\ts_mov_b32 %0, %5\n\tbuffer_load_dwordx4 %3, %4, %0 offen sc1 lds" : "=&s"(t) : "s"(m0base), "i"(LOFF), "v"(voff), "s"(srd), "i"(SOFF) : "scc", "memory");
  else
  asm volatile("s_add_u32 m0, %1, %2\n\ts_mov_b32 %0, %5\n\tbuffer_load_dwordx4 %3, %4, %0 offen lds" : "=&s"(t) : "s"(m0base), "i"(LOFF), "v"(voff), "s"(srd), "i"(SOFF) : "scc", "memory");
}
template <int LOFF, int SOFF> DI unsigned dma_prep(unsigned m0base) { unsigned t; asm volatile("s_add_u32 m0, %1, %2\n\ts_mov_b32 %0, %3" : "=s"(t) : "s"(m0base), "i"(LOFF), "i"(SOFF) : "scc"); return t; }
template <int LOFF> DI void m0_set(unsigned m0base) { asm volatile("s_add_u32 m0, %0, %1" :: "s"(m0base), "i"(LOFF) : "scc"); }
template <int V> DI unsigned s_imm() { unsigned t; asm volatile("s_mov_b32 %0, %1" : "=s"(t) : "i"(V)); return t; }
template <bool SC1 = false, int IMM = 0> DI void dma_go(unsigned soff, unsigned voff, rsrc_t srd) {
  if constexpr (SC1) asm volatile("buffer_load_dwordx4 %0, %1, %2 offen offset:%3 sc1 lds" :: "v"(voff), "s"(srd), "s"(soff), "i"(IMM) : "memory");
  else               asm volatile("buffer_load_dwordx4 %0, %1, %2 offen offset:%3 lds" :: "v"(voff), "s"(srd), "s"(soff), "i"(IMM) : "memory");
}
DI bf16x8 ld128(const LAS char* p) { return *(const LAS bf16x8*)p; }
DI s16x4 ldtr(const LAS char* p) { return __builtin_bit_cast(s16x4, __builtin_amdgcn_ds_read_tr16_b64_v4i16((LAS i16x4*)p)); }
DI bf16x8 cat(s16x4 a, s16x4 b) { return __builtin_shufflevector(a, b, 0, 1, 2, 3); }
DI u32x4 pack8(const f32x4& x, const f32x4& y) { u32x4 w; w[0] = cvtpk(x[0], x[1]); w[1] = cvtpk(x[2], x[3]); w[2] = cvtpk(y[0], y[1]); w[3] = cvtpk(y[2], y[3]); return w; }

#define ACLOB "a0","a1","a2","a3","a4","a5","a6","a7","a8","a9","a10","a11","a12","a13","a14","a15","a16","a17","a18","a19","a20","a21","a22","a23","a24","a25","a26","a27","a28","a29","a30","a31","a32","a33","a34","a35","a36","a37","a38","a39","a40","a41","a42","a43","a44","a45","a46","a47","a48","a49","a50","a51","a52","a53","a54","a55","a56","a57","a58","a59","a60","a61","a62","a63","a64","a65","a66","a67","a68","a69","a70","a71","a72","a73","a74","a75","a76","a77","a78","a79","a80","a81","a82","a83","a84","a85","a86","a87","a88","a89","a90","a91","a92","a93","a94","a95","a96","a97","a98","a99","a100","a101","a102","a103","a104","a105","a106","a107","a108","a109","a110","a111","a112","a113","a114","a115","a116","a117","a118","a119","a120","a121","a122","a123","a124","a125","a126","a127","a128","a129","a130","a131","a132","a133","a134","a135","a136","a137","a138","a139","a140","a141","a142","a143","a144","a145","a146","a147","a148","a149","a150","a151","a152","a153","a154","a155","a156","a157","a158","a159","a160","a161","a162","a163","a164","a165","a166","a167","a168","a169","a170","a171","a172","a173","a174","a175","a176","a177","a178","a179","a180","a181","a182","a183","a184","a185","a186","a187","a188","a189","a190","a191","a192","a193","a194","a195","a196","a197","a198","a199","a200","a201","a202","a203","a204","a205","a206","a207","a208","a209","a210","a211","a212","a213","a214","a215","a216","a217","a218","a219","a220","a221","a222","a223","a224","a225","a226","a227","a228","a229","a230","a231","a232","a233","a234","a235","a236","a237","a238","a239","a240","a241","a242","a243","a244","a245","a246","a247","a248","a249","a250","a251","a252","a253","a254","a255"
#define AFENCE() asm volatile("" ::: ACLOB)
template <int R> DI float aread1() { float a; asm volatile("v_accvgpr_read_b32 %0, a[%c1]" : "=v"(a) : "i"(R)); return a; }
template <int R> DI f32x4 aread4() { float a, b, c, d;
  asm volatile("v_accvgpr_read_b32 %0, a[%c4]\n\tv_accvgpr_read_b32 %1, a[%c5]\n\tv_accvgpr_read_b32 %2, a[%c6]\n\tv_accvgpr_read_b32 %3, a[%c7]" : "=v"(a), "=v"(b), "=v"(c), "=v"(d) : "i"(R), "i"(R + 1), "i"(R + 2), "i"(R + 3));
  return f32x4{a, b, c, d}; }
template <int R> DI void awrite4(const f32x4& v) {
  asm volatile("v_accvgpr_write_b32 a[%c4], %0\n\tv_accvgpr_write_b32 a[%c5], %1\n\tv_accvgpr_write_b32 a[%c6], %2\n\tv_accvgpr_write_b32 a[%c7], %3" :: "v"(v[0]), "v"(v[1]), "v"(v[2]), "v"(v[3]), "i"(R), "i"(R + 1), "i"(R + 2), "i"(R + 3)); }
template <int R> DI void azero4() { asm volatile("v_accvgpr_write_b32 a[%c0], 0\n\tv_accvgpr_write_b32 a[%c1], 0\n\tv_accvgpr_write_b32 a[%c2], 0\n\tv_accvgpr_write_b32 a[%c3], 0" :: "i"(R), "i"(R + 1), "i"(R + 2), "i"(R + 3)); }
template <int R> DI void azero2() { asm volatile("v_accvgpr_write_b32 a[%c0], 0\n\tv_accvgpr_write_b32 a[%c1], 0" :: "i"(R), "i"(R + 1)); }
template <int R, class TA, class TB> DI void mma_acc(const TA& a, const TB& b) { asm volatile("v_mfma_f32_16x16x32_bf16 a[%c2:%c3], %0, %1, a[%c2:%c3]" :: "v"(a), "v"(b), "i"(R), "i"(R + 3)); }
template <int R, class TA, class TB> DI void mma_acc0(const TA& a, const TB& b) { asm volatile("v_mfma_f32_16x16x32_bf16 a[%c2:%c3], %0, %1, 0" :: "v"(a), "v"(b), "i"(R), "i"(R + 3)); }
#define MMA_V(acc, a, b) asm volatile("v_mfma_f32_16x16x32_bf16 %0, %1, %2, %0" : "+v"(acc) : "v"(a), "v"(b))
#define MMA_V0(acc, a, b) asm volatile("v_mfma_f32_16x16x32_bf16 %0, %1, %2, 0" : "=&v"(acc) : "v"(a), "v"(b))
DI unsigned cvt_asm(float lo, float hi) { unsigned w; asm volatile("v_cvt_pk_bf16_f32 %0, %1, %2" : "=v"(w) : "v"(lo), "v"(hi)); return w; }
DI float exp_asm(float x) { float r; asm volatile("v_exp_f32 %0, %1" : "=v"(r) : "v"(x)); return r; }
DI float mul_asm(float a, float b) { float r; asm volatile("v_mul_f32 %0, %1, %2" : "=v"(r) : "v"(a), "v"(b)); return r; }
template <int R, class TB> DI void mma_ab0(f32x4& d, const TB& b) { asm volatile("v_mfma_f32_16x16x32_bf16 %0, a[%c2:%c3], %1, 0" : "=&v"(d) : "v"(b), "i"(R), "i"(R + 3)); }
DI void st16_sc1(const u32x4& w, unsigned voff, rsrc_t srd, unsigned soff) { __builtin_amdgcn_raw_buffer_store_b128(w, srd, voff, soff, 16); }
#define KEEP(x) asm volatile("" :: "v"(x))
DI void st16_pad(const u32x4& w, unsigned voff, rsrc_t srd, unsigned soff) { st16_sc1(w, voff, srd, soff); asm volatile("s_nop 1" :: "v"(w) : "memory"); }
template <int P, int LOFF> DI void dma_piece(unsigned m0base, unsigned voff, rsrc_t srd, unsigned soff) {
  if constexpr ((P & 3) == 0) asm volatile("s_add_u32 m0, %0, %4\n\ts_nop 0\n\tbuffer_load_dwordx4 %1, %2, %3 offen sc1 lds" :: "s"(m0base), "v"(voff), "s"(srd), "s"(soff + P * 1024), "i"(LOFF + P * 1024) : "scc", "memory");
  else asm volatile("buffer_load_dwordx4 %0, %1, %2 offen offset:%3 sc1 lds" :: "v"(voff), "s"(srd), "s"(soff + (P & 4) * 1024), "i"((P & 3) * 1024) : "memory");
}

constexpr size_t WS_Q = 0, WS_K = 16u << 20, WS_V = 32u << 20, WS_PART = 68u << 20, WS_KVP = 82u << 20, WS_SYNC = 84u << 20, WS_W = 102u << 20, WS_L = 107u << 20, WS_KVF = 109u << 20, WS_END = 110u << 20, WS_XCH = 112u << 20;
constexpr int WIMG = 262144, LIMG = 86016;
constexpr unsigned CODE_BYTES = 105984;
DI unsigned code_line(unsigned wave128, unsigned k) { return min((wave128 + min(k, 6u) * 128) * 128, CODE_BYTES - 128); }
constexpr float QSCALE = 0.09016844005556021f;
typedef unsigned long long u64;
constexpr u64 FMAGIC = 0x5AA5C33C00000000ull, FBUMP = 1024;
enum { F_KV = 0, F_LIN = 2, F_PACK = 3, F_Q = 4, F_PART = 5, F_SUM = 6, F_OUT = 7   };
constexpr int SY_CTRH = 0, SY_CTRR = 4096;
DI unsigned fl_off(unsigned head, unsigned j, unsigned wave, unsigned kind) { return 8192u + ((head * 16 + j) * 4 + wave) * 2048u + kind * 128u; }
DI bool flag_ok(u64 f, u64 tk) { return ((f ^ FMAGIC) - tk + 16ull) < 32ull; }
DI u32x4 flag_word(u64 tk) { const unsigned lo = (unsigned)tk, hi = (unsigned)((tk ^ FMAGIC) >> 32); return u32x4{lo, hi, lo, hi}; }
#define FLAG_STORE(off, val, base) "s_mov_b64 exec, 0xff\n\tglobal_store_dwordx4 " off ", " val ", " base " sc1\n\ts_mov_b64 exec, -1\n\ts_nop 0"
DI void flag_set1(char* p, unsigned off, u64 tk, unsigned skip, unsigned voff) {
  asm volatile("s_cmp_lg_u32 %3, 0\n\ts_cbranch_scc1 1f\n\t" FLAG_STORE("%0", "%1", "%2") "\n1:" :: "v"(voff + off), "v"(flag_word(tk)), "s"(p), "s"(skip) : "scc", "memory");
}
DI void flag_set0(char* p, unsigned off, u64 tk, unsigned voff) { asm volatile(FLAG_STORE("%0", "%1", "%2") :: "v"(voff + off), "v"(flag_word(tk)), "s"(p) : "memory"); }
DI void ctr_bump1(char* p, unsigned skip) {
  asm volatile("s_cmp_lg_u32 %3, 0\n\ts_cbranch_scc1 1f\n\ts_mov_b64 exec, 1\n\tglobal_atomic_add_x2 %0, %1, %2\n\ts_mov_b64 exec, -1\n1:" :: "v"(0u), "v"(FBUMP), "s"(p), "s"(skip) : "scc", "memory");
}
DI void flag_wait(const char* p, bool act, u64 tk) {
#pragma nounroll
  for (unsigned spin = 0; spin < (1u << 16); ++spin) {
    const u64 f = __hip_atomic_load((GAS u64*)p, __ATOMIC_RELAXED, __HIP_MEMORY_SCOPE_AGENT);
    if (__builtin_amdgcn_ballot_w64(act && !flag_ok(f, tk)) == 0ull) break;
    __builtin_amdgcn_s_sleep(1);
  }
  asm volatile("" ::: "memory");
}

struct Wg {
  char* ws; const char* code; const LAS char* lds; unsigned lds0;
  unsigned lane, wid, warm;
  int head, rb, p, j; bool roleA;
  u64 tkH, tkR;
  u32x4 pq[2]; float g[2];
};
DI void wg_fresh(Wg& w) { asm volatile("" : "+s"(w.ws), "+s"(w.wid), "+s"(w.head), "+s"(w.j)); w.p = w.j & 7; w.roleA = w.j >= 8; w.rb = w.roleA ? 15 - w.p : w.p; }

constexpr int XSTR = 2 << 20;
struct Op {
  const LAS char* wb[3][4];
  unsigned xo, g0, u0;
  rsrc_t srdW, srdX;
};
template <int NT> DI void op_bases(Op& o, const Wg& w, unsigned lane) {
  const unsigned g = lane >> 4, q = (lane >> 2) & 3;
#pragma unroll
  for (int hb = 0; hb < 3; ++hb) { o.wb[hb][NT] = w.lds + (hb * 65536 + (4 * g + q) * 128 + (((NT >> 1) ^ (q >> 1)) * 64) + (lane & 3) * 16 + (((NT ^ g ^ w.head) & 1) * 8)); PIN_V(o.wb[hb][NT]); }
}
DI void op_init(Op& o, const Wg& w) {
  o.srdW = mk_rsrc(w.ws + WS_W + w.head * WIMG + w.wid * 8192, WIMG);
  o.srdX = mk_rsrc(w.ws + WS_XCH + w.j * 131072 + w.wid * 28672, 15u * XSTR + 28672u);
  o.xo = w.head * XSTR; o.u0 = (w.head + 4 * w.wid) & 15; o.g0 = o.u0 * 1024;
}
template <int R, bool FIRST> DI void sum1(f32x4& d, const bf16x8& sel) {
  if constexpr (FIRST) asm volatile("v_mfma_f32_16x16x32_bf16 %0, %1, a[%c2:%c3], 0" : "=&v"(d) : "v"(sel), "i"(R), "i"(R + 3));
  else                 asm volatile("v_mfma_f32_16x16x32_bf16 %0, %1, a[%c2:%c3], %0" : "+v"(d) : "v"(sel), "i"(R), "i"(R + 3));
}
template <int R0, int... Is> DI void sum_unit(f32x4& d0, f32x4& d1, const bf16x8& se, const bf16x8& so, const bf16x8& sb, iseq<Is...>) {
  sum1<R0, true>(d0, se);
  ((sum1<R0 + 8 * Is + 2, Is == 0>(d1, sb), sum1<R0 + (Is < 7 ? 8 * Is + 6 : 60), false>(d0, Is < 7 ? sb : so)), ...);
}
template <int I> DI void gather1(rsrc_t srd, unsigned voff, unsigned g0, unsigned mask) {
  unsigned t;
  asm volatile("s_bitcmp0_b32 %4, %5\n\ts_cbranch_scc1 1f\n\ts_add_u32 %0, %3, %6\n\tbuffer_load_dwordx4 a[%c7:%c8], %1, %2, %0 offen sc1\n1:"
               : "=&s"(t) : "v"(voff), "s"(srd), "s"(g0), "s"(mask), "i"(I), "i"((I & 15) * XSTR + (I >> 4) * 16384), "i"(4 * I), "i"(4 * I + 3) : "scc", "memory");
}
template <int... Is> DI void gather_all(rsrc_t srd, unsigned voff, unsigned g0, unsigned mask, iseq<Is...>) { (gather1<Is>(srd, voff, g0, mask), ...); }
DI void out_unit(f32x4& d0, f32x4& d1, unsigned u, unsigned lane, const Wg& w, u16* out) {
  st16_pad(pack8(d0, d1), (lane & 15) * 896 + (lane >> 4) * 16, mk_rsrc(out + (size_t)(w.rb * 128 + w.wid * 16) * 448, 80 * 896), ((u >> 1) & 1) * 64 * 896 + (u >> 2) * 128 + (u & 1) * 64);
}

#define T0N 1
#define TDIST 3
constexpr int STAGE = 65536, BOFF = 32768;
struct KVCtx {
  const LAS char* abase[2][2];
  const LAS char* bbase[2][2];
  unsigned vA[2], vB[2];
  unsigned m0, so;
  rsrc_t srdA, srdB; unsigned pfd, pfe;
  u64 tk, flp;
  const char* cp; const char* cb; unsigned cv, cstep, clim, dv, dstep, dlim, dcur; bool fix, swp; u32x4 cd, ce; rsrc_t srdD;
  const char* tp0; const char* tb; unsigned tv, tstep, tlim;
};
struct XCtx {
  unsigned vA[2], vQ, vS[4], voff;
  unsigned m0Q, m0L, m0K, s1, ksoff[2];
  rsrc_t srdA, srdQ, srdL, srdG, srdS, srdK;
};
struct XRd { const LAS char* xa[2][2]; const LAS char* lb[2][2]; const LAS char* qb[2]; const LAS char* gb; };
template <int SLOT, int P> DI void dmaA(const KVCtx& c) { dma16i<SLOT * STAGE + P * 1024, P * 8 * 2048>(c.m0, c.vA[P & 1], c.srdA); }
template <int SLOT, int P> DI void dmaB(const KVCtx& c) { dma16i<SLOT * STAGE + BOFF + P * 1024, (P >> 1) * 4 * 8192 + (P & 1) * 256>(c.m0, c.vB[P >> 2], c.srdB); }
template <int BASE, int T, int P> DI void dmaXL(const XCtx& x) {
  if constexpr (P < 4) dma16i<BASE + P * 1024, T * 128 + P * 8 * 896>(x.m0Q, x.vA[P & 1], x.srdA);
  else                 dma16i<BASE + 16384 + (P - 4) * 1024, T * 12288 + (P - 4) * 1024, true>(x.m0L, x.voff, x.srdL);
}
template <int BASE, int K, int P> DI void dmaQW(const XCtx& x) { dma16i<BASE + P * 1024, K * 32 * 8192 + (P >> 1) * 4 * 8192 + (P & 1) * 256>(x.m0Q, x.vQ, x.srdQ); }

template <bool VT, int NSLOT, int NK, int DMA, int DSLOT, int G>
DI void kv_gap(KVCtx& c, const XCtx& x, unsigned lds0, const bf16x8 (&ca)[8], const bf16x8 (&cb)[8], bf16x8 (&na)[8], bf16x8 (&nb)[8], s16x4& lo) {
  if constexpr (G == 0 || G == 32) AFENCE();
  if constexpr (G == 0 && NK == 1) LGKM(0);
  constexpr int ia = G & 7, ib = G >> 3;
  constexpr int R = 4 * (8 * ib + ia);
  if constexpr (DMA == 5) { if constexpr (VT) mma_acc0<R>(ca[ia], cb[ib]); else mma_acc0<R>(cb[ia], ca[ib]); }
  else                    { if constexpr (VT) mma_acc<R>(ca[ia], cb[ib]);  else mma_acc<R>(cb[ia], ca[ib]); }
  if constexpr ((G & 1) == 0 && G < 48) {
    constexpr int u = G >> 1;
    if constexpr (u % 3 == 0) na[u / 3] = ld128(c.abase[NSLOT][NK] + (u / 3) * 2048);
    else { constexpr int n = u / 3, t = u % 3 - 1; const s16x4 v = ldtr(c.bbase[NSLOT][n & 1] + NK * 16384 + (n >> 1) * 256 + t * 2048);
      if constexpr (t == 0) lo = v; else nb[n] = cat(lo, v); }
  }
  if constexpr (DMA == 1) {
    { constexpr int p = G >> 2, P = p >> 1;
      if constexpr ((G & 3) == 1) { if constexpr (p & 1) c.so = dma_prep<DSLOT * STAGE + BOFF + P * 1024, (P >> 1) * 4 * 8192 + (P & 1) * 256>(c.m0); else c.so = dma_prep<DSLOT * STAGE + P * 1024, P * 8 * 2048>(c.m0); }
      if constexpr ((G & 3) == 3) { if constexpr (p & 1) dma_go(c.so, c.vB[P >> 2], c.srdB); else dma_go(c.so, c.vA[P & 1], c.srdA); } }
    if constexpr (G == 56) asm volatile("s_waitcnt vmcnt(15)" : "+v"(c.cd) :: "memory");
    if constexpr (G == 58) { c.ce[0] = c.swp ? c.cd[2] : c.cd[0]; c.ce[1] = c.swp ? c.cd[3] : c.cd[1]; PIN_V(c.ce[0]); PIN_V(c.ce[1]); }
    if constexpr (G == 60) { c.ce[2] = c.swp ? c.cd[0] : c.cd[2]; c.ce[3] = c.swp ? c.cd[1] : c.cd[3]; PIN_V(c.ce[2]); PIN_V(c.ce[3]); }
    if constexpr (G == 62) { c.ce[2] = c.fix ? 0u : c.ce[2]; c.ce[3] = c.fix ? 0u : c.ce[3]; PIN_V(c.ce[2]); PIN_V(c.ce[3]); }
  }
  if constexpr (DMA == 5 && G < 48 && G % 3 == 1) { constexpr int p = G / 3; if constexpr (p & 1) dmaB<1, (p >> 1)>(c); else dmaA<1, (p >> 1)>(c); }
  if constexpr (DMA == 4 || DMA == 5) {
    if constexpr (DMA == 4) {
      if constexpr (G == 3) asm volatile("global_load_dword %0, %1, off" : "+v"(c.pfe) : "v"(c.tp0) : "memory");
      if constexpr (G == 7) asm volatile("global_load_dwordx4 %0, %1, off" : "+v"(c.cd) : "v"(c.cp) : "memory");
      if constexpr (G == 13) st16_sc1(c.ce, c.dcur, c.srdD, 0);
      if constexpr (G == 15) KEEP(c.ce);
      if constexpr (G == 35) { c.dcur = c.dv | ((c.dlim - c.dv) & 0x80000000u); PIN_V(c.dcur); }
      if constexpr (G == 37) { c.dv += c.dstep; PIN_V(c.dv); }
      if constexpr (G == 39) { c.cv = min(c.cv + c.cstep, c.clim); PIN_V(c.cv); }
      if constexpr (G == 41) { c.cp = c.cb + c.cv; PIN_V(c.cp); }
    }
    if constexpr (G == 61) { c.tp0 = c.tb + c.tv; PIN_V(c.tp0); }
    if constexpr (G == 63) { c.tv = min(c.tv + c.tstep, c.tlim); PIN_V(c.tv); }
    if constexpr (G == 51 || G == 53) asm volatile("v_add_u32 %0, 0x80, %0" : "+v"(c.vA[(G >> 1) & 1]));
    if constexpr (G == 55 || G == 57) asm volatile("v_add_u32 %0, 0x80000, %0" : "+v"(c.vB[(G >> 1) & 1]));
  }
  if constexpr (DMA == 2 && (G & 3) == 1) { constexpr int p = G >> 2;
    if constexpr (p < 7) dmaXL<131072, 0, p>(x);
    else if constexpr (p < 11) dmaQW<28672, 0, p - 7>(x);
    else if constexpr (p == 11) dma16i<61440, 0>(lds0, x.voff, x.srdG);
    else dmaQW<45056, 1, p - 12>(x); }
  if constexpr (DMA == 3 && (G & 7) == 1 && (G >> 3) < 7) dmaXL<0, 1, (G >> 3)>(x);
  SBAR();
}
template <bool VT, int NSLOT, int NK, int DMA, int DSLOT, int... Gs>
DI void kstep_(KVCtx& c, const XCtx& x, unsigned lds0, const bf16x8 (&ca)[8], const bf16x8 (&cb)[8], bf16x8 (&na)[8], bf16x8 (&nb)[8], iseq<Gs...>) { s16x4 lo; (kv_gap<VT, NSLOT, NK, DMA, DSLOT, Gs>(c, x, lds0, ca, cb, na, nb, lo), ...); }
template <bool VT, int NSLOT, int NK, int DMA, int DSLOT>
DI void kstep(KVCtx& c, const XCtx& x, unsigned lds0, const bf16x8 (&ca)[8], const bf16x8 (&cb)[8], bf16x8 (&na)[8], bf16x8 (&nb)[8]) { kstep_<VT, NSLOT, NK, DMA, DSLOT>(c, x, lds0, ca, cb, na, nb, make_iseq<64>{}); }

#define WAIT_KV() WAIT_BAR(3)
template <bool VT>
DI void kv_loop(Wg& w, KVCtx& c, const XCtx& x, unsigned lds0, bf16x8 (&fa)[8], bf16x8 (&fb)[8]) {
  bf16x8 a0[8], b0[8], a1[8], b1[8];
  c.pfe = 0;
  asm volatile("global_load_dword %0, %1, off" : "+v"(c.pfe) : "v"(c.tp0) : "memory");
  asm volatile("s_waitcnt vmcnt(1)\n\ts_barrier" :: "v"(c.pfd), "v"(w.warm) : "memory");
#pragma unroll
  for (int i = 0; i < 8; ++i) { a0[i] = ld128(c.abase[0][0] + i * 2048); b0[i] = cat(ldtr(c.bbase[0][i & 1] + (i >> 1) * 256), ldtr(c.bbase[0][i & 1] + (i >> 1) * 256 + 2048)); }
  SBAR();
  kstep<VT, 0, 1, 5, 0>(c, x, lds0, a0, b0, a1, b1);
  char* sy = w.ws + WS_SYNC;
  asm volatile("s_waitcnt vmcnt(0)\n\ts_cmp_lg_u32 %2, 3\n\ts_cbranch_scc1 1f\n\ts_mov_b64 exec, 3\n\tglobal_atomic_add_x2 %0, %1, %0, off sc0\n\ts_mov_b64 exec, -1\n1:"
               : "+v"(c.tk) : "v"(w.lane == 0 ? sy + SY_CTRH + w.head * 256 : sy + SY_CTRR + w.rb * 256), "s"(w.wid) : "scc", "memory");
  for (int t = 0; t < 14; t += 2) {
    WAIT_KV();
    kstep<VT, 1, 0, 1, 0>(c, x, lds0, a1, b1, a0, b0);
    kstep<VT, 1, 1, 4, 0>(c, x, lds0, a0, b0, a1, b1);
    WAIT_KV();
    kstep<VT, 0, 0, 1, 1>(c, x, lds0, a1, b1, a0, b0);
    if (t == 8) {
      PIN_V(c.tk);
      if (w.wid == 3 && w.lane < 2) { *(LAS u64*)((LAS char*)w.lds + 163824 + w.lane * 8) = c.tk; if (w.lane == 0) __hip_atomic_store((GAS u64*)(sy + fl_off(w.head, w.j, 3, F_PACK)), c.tk ^ FMAGIC, __ATOMIC_RELAXED, __HIP_MEMORY_SCOPE_AGENT); }
    }
    asm volatile("s_cmp_lg_u32 %2, 12\n\ts_cbranch_scc1 1f\n\tglobal_load_dwordx2 %0, %1, off sc1\n1:" : "+v"(c.flp) : "v"(sy + fl_off(w.head, w.lane & 15, 3, F_PACK)), "s"(t) : "scc", "memory");
    kstep<VT, 0, 1, 4, 0>(c, x, lds0, a0, b0, a1, b1);
  }
  { const u32x4 t_ = *(const LAS u32x4*)(w.lds + 163824); w.tkH = ((u64)rfl(t_[1]) << 32) | rfl(t_[0]); w.tkR = ((u64)rfl(t_[3]) << 32) | rfl(t_[2]); }
  WAIT_KV();
  PIN_V(c.flp);
  if (__builtin_amdgcn_ballot_w64(!flag_ok(c.flp, w.tkH)) != 0ull) flag_wait(sy + fl_off(w.head, w.lane & 15, 3, F_PACK), true, w.tkH);
  asm volatile("s_nop 1" ::: "memory");
  kstep<VT, 1, 0, 2, 0>(c, x, lds0, a1, b1, a0, b0);
  kstep<VT, 1, 1, 3, 0>(c, x, lds0, a0, b0, a1, b1);
  WAIT_BAR(11);
  asm volatile("" :: "v"(c.pfe), "v"(c.cd));
#pragma unroll
  for (int i = 0; i < 8; ++i) { fa[i] = VT ? a1[i] : b1[i]; fb[i] = VT ? b1[i] : a1[i]; }
}
DI f32x4 phi4(const f32x4& v) {
  const f32x4 u = v * 1.4426950408889634f, t = v + 1.f; float e0, e1, e2, e3;
  asm("v_exp_f32 %0, %4 clamp\n\tv_exp_f32 %1, %5 clamp\n\tv_exp_f32 %2, %6 clamp\n\tv_exp_f32 %3, %7 clamp\n\tv_max_f32 %0, %0, %8\n\tv_max_f32 %1, %1, %9\n\tv_max_f32 %2, %2, %10\n\tv_max_f32 %3, %3, %11"
      : "=&v"(e0), "=&v"(e1), "=&v"(e2), "=&v"(e3) : "v"(u[0]), "v"(u[1]), "v"(u[2]), "v"(u[3]), "v"(t[0]), "v"(t[1]), "v"(t[2]), "v"(t[3]));
  return f32x4{e0, e1, e2, e3} + 1e-6f;
}
DI u32x2 pack4(const f32x4& x) { return u32x2{cvtpk(x[0], x[1]), cvtpk(x[2], x[3])}; }
template <int A, int P> DI void k_first(const XCtx& x) { dma_piece<P, 65536 + A * 32768>(x.m0K, x.voff, x.srdK, x.ksoff[A]); SBAR(); }
template <int R, int OFF> DI void lds_to_a(const LAS char* p) { asm volatile("ds_read_b128 a[%c1:%c2], %0 offset:%c3" :: "v"(p), "i"(R), "i"(R + 3), "i"(OFF) : "memory"); }

template <int R0, int... Rs> DI void zero_O(const LAS char* z, iseq<Rs...>) { (lds_to_a<R0 + 4 * Rs, 0>(z), ...); SBAR(); }
template <int N> DI void q_out1(const XCtx& x, const f32x4 (&aq)[16][2], u32x4 (&qf)[16], rsrc_t srd) {
  constexpr int s = N >> 1, m = N & 1;
  qf[N] = pack8(aq[2 * s][m] * QSCALE, aq[2 * s + 1][m] * QSCALE);
  st16_sc1(qf[N], x.voff + (s & 3) * 1024, srd, m * 8192 + (s >> 2) * 4096); SBAR();
}
template <int R0, int... Ns> DI void q_to_a(const u32x4 (&qf)[16], iseq<Ns...>) { (awrite4<R0 + 32 * (Ns & 1) + 4 * (Ns >> 1)>(__builtin_bit_cast(f32x4, qf[Ns])), ...); }
struct In { const u16 *x, *ckv, *wq, *wuk, *wuv, *wo, *wqkv, *bwo, *gw; u16* out; };

DI void phase1(Wg& w, const In& in) {
  const unsigned lane = w.lane, wid = w.wid, wm = wid >> 1, wn = wid & 1, r = lane & 15, g = lane >> 4, lds0 = w.lds0;
  const LAS char* lds = w.lds;
  const int rt = w.j & 7, head = w.head; const bool isv = w.j >= 8;
  char* sy = w.ws + WS_SYNC;
  KVCtx c; XCtx x;
  c.pfd = 0;
  asm volatile("global_load_dword %0, %1, %2" : "+v"(c.pfd) : "v"(code_line((blockIdx.x >> 3) * 4 + (threadIdx.x >> 6), lane)), "s"(w.code) : "memory");
  SBAR();
  asm volatile("" :: "s"(in.gw), "s"(w.ws), "s"(in.out), "s"(in.x));
  const char* tb0 = (const char*)in.ckv + (size_t)(rt * 256 + ((head & 1) * 2 + (isv ? 1 : 0)) * 64) * 2048;
  const char* tb1 = (const char*)(isv ? in.wuv : in.wuk) + (size_t)(rt * 8) * 8192 + head * 512;
  { const unsigned tt = 1 + min((lane >> 3) & 3, (unsigned)(T0N - 1));
    const char* pa = tb0 + ((head >> 1) * 8 + wid * 2 + (lane & 1)) * 2048 + tt * 128; const char* pb = tb1 + (tt * 64 + wid * 2 + ((lane >> 2) & 1)) * 8192 + (lane & 3) * 128;
    asm volatile("global_load_dword %0, %1, off" : "+v"(c.pfd) : "v"(lane < 32 ? pa : pb) : "memory"); }
  SBAR();
#pragma unroll
  for (int s = 0; s < 2; ++s) { c.abase[0][s] = lds + ((wm * 128 + r) * 128 + (((4 * s + g) ^ ((r >> 1) & 7)) << 4)); c.abase[1][s] = c.abase[0][s] + STAGE; PIN_V(c.abase[0][s]); PIN_V(c.abase[1][s]); }
#pragma unroll
  for (int par = 0; par < 2; ++par) { c.bbase[0][par] = lds + (BOFF + wn * 1024 + g * 4096 + ((lane >> 2) & 3) * 64 + ((par ^ (g & 1)) * 32) + (lane & 3) * 8); c.bbase[1][par] = c.bbase[0][par] + STAGE; PIN_V(c.bbase[0][par]); PIN_V(c.bbase[1][par]);
    c.vA[par] = (wid * 64 + (lane >> 3)) * 2048 + (((lane & 7) ^ (4 * par + (lane >> 4))) << 4);
    c.vB[par] = (wid * 16 + ((lane >> 2) & 3)) * 8192 + (lane >> 4) * 64 + ((lane & 3) ^ (2 * par)) * 16; }
  c.m0 = lds0 + wid * 8192;
  c.srdA = mk_rsrc(in.ckv + (size_t)rt * 256 * 1024, 256 * 2048);
  c.srdB = mk_rsrc((isv ? in.wuv : in.wuk) + head * 256, 1024 * 8192 - head * 512);
  auto tile0 = [&](int q) { const unsigned pa = (q + (head & 1) * 2 + (isv ? 1 : 0)) & 3, pb = (q + rt) & 3, vb = pb >= 2 ? c.vB[1] : c.vB[0];
    dma16(c.m0 + pa * 2048, c.vA[0], c.srdA, pa * 32768); dma16(c.m0 + BOFF + pb * 2048, vb, c.srdB, pb * 32768);
    dma16(c.m0 + pa * 2048 + 1024, c.vA[1], c.srdA, pa * 32768 + 16384); dma16(c.m0 + BOFF + pb * 2048 + 1024, vb, c.srdB, pb * 32768 + 256); };
  tile0(0); tile0(1);
  SBAR();
  {
    const unsigned j = w.j, iq = j * 8 + wid * 2 + (lane & 1);
    u64 pb[8] = {(u64)tb0, (u64)tb1, (u64)in.x, (u64)in.wq, (u64)w.code, (u64)in.wo, (u64)in.wqkv, (u64)in.bwo};
    const unsigned iw = min(wid * 32 + lane, 111u), rw = iw >= 56 ? 1 : 0, cw = iw - rw * 56, part = min(wid, 2u), cl = (lane & 3) ^ (j & 2), cb = min(wid * 16 + (lane & 15), 55u), sw = (j & 1) * 4;
    unsigned pv[8] = {((head >> 1) * 8 + wid * 2 + (lane & 1)) * 2048 + (TDIST + 1) * 128, ((TDIST + 1) * 64 + wid * 2 + ((lane >> 2) & 1)) * 8192 + (lane & 3) * 128, (w.rb * 128 + head * 8 + wid * 2) * 896,
                      (iq >> 2) * 8192 + head * 512 + (iq & 3) * 128, min((((head & 1) * 16 + j) * 4 + wid) * 896, CODE_BYTES - 896),
                      (head * 256 + j * 2 + rw) * 896 + cw * 16, (j * 4 + ((lane >> 2) & 3)) * 2688 + part * 896 + head * 56 + min(cl * 16, 40u), (head * 28 + min(j * 2, 26u)) * 896 + cb * 16};
    unsigned pd[3] = {head * WIMG + ((cw >> 3) * 9 + 1) * 4096 + (j * 2 + rw) * 128 + ((cw & 7) ^ sw) * 16, (unsigned)(WS_L - WS_W) + head * LIMG + (j * 3 + part) * 256 + (lane & 15) * 16,
                      head * WIMG + (cb >> 3) * 9 * 4096 + j * 256 + ((cb & 7) ^ sw) * 16};
    const unsigned ps[5] = {128, 64 * 8192, 128, 32 * 8192, 128}, pn[5] = {14 - TDIST, 14 - TDIST, 13, 13, 6}, pl[4] = {16, 24, 32, 34};
#pragma unroll
    for (int q = 0; q < 8; ++q) { asm volatile("" : "+s"(pb[q])); PIN_V(pv[q]); }
    u64 b = pb[4]; unsigned v = pv[4], st = ps[4], sn = ps[4] * pn[4];
#pragma unroll
    for (int q = 3; q >= 0; --q) { const bool k = lane < pl[q]; b = k ? pb[q] : b; v = k ? pv[q] : v; st = k ? ps[q] : st; sn = k ? ps[q] * pn[q] : sn; }
    if (lane >= 62) { b = (u64)w.ws; v = lane == 62 ? (unsigned)WS_SYNC + fl_off(head, j, wid, 0) : (unsigned)WS_KVP + (head * 16 + w.rb) * 4096 + wid * 1024; sn = lane == 62 ? 1152 : 896; }
    c.tb = (const char*)b; c.tv = v; c.tstep = st; c.tlim = v + sn;
    const bool k0 = lane < 32, k1 = lane < 48, on = wid < 3 || lane < 16 || (lane >= 48 && lane < 56);
    c.cb = (const char*)(k0 ? pb[5] : k1 ? pb[6] : pb[7]); c.cv = k0 ? pv[5] : k1 ? pv[6] : pv[7];
    c.cstep = k0 ? 32 * 896 : k1 ? 64 * 2688 : 896; c.clim = c.cv + (k0 ? 7 * 32 * 896 : k1 ? 6 * 64 * 2688 : 896);
    c.dv = k0 ? pd[0] : k1 ? pd[1] : pd[2]; c.dstep = k0 ? 4096 : k1 ? 12288 : 128; c.dlim = on ? c.dv + (k0 ? 7 * 4096 : k1 ? 6 * 12288 : 128) : 0u;
    c.dcur = 0x7ffffff0u; c.fix = !k0 && k1 && cl == 3; c.swp = c.fix || ((k0 || !k1) && (j & 2)); c.cp = c.cb + c.cv; c.cd = u32x4{}; c.srdD = mk_rsrc(w.ws + WS_W, (unsigned)(WS_KVF - WS_W));
    const unsigned t23 = min(T0N + 1 + ((lane >> 3) & 3), (unsigned)(TDIST));
    c.tp0 = lane < 32 ? tb0 + ((head >> 1) * 8 + wid * 2 + (lane & 1)) * 2048 + t23 * 128 : tb1 + (t23 * 64 + wid * 2 + ((lane >> 2) & 1)) * 8192 + (lane & 3) * 128; }
  c.tk = 1; c.flp = 0;
#pragma unroll
  for (int par = 0; par < 2; ++par) x.vA[par] = (wid * 32 + (lane >> 3)) * 896 + (((lane & 7) ^ (4 * par + (lane >> 4))) << 4);
  x.vQ = (wid * 8 + ((lane >> 2) & 3)) * 8192 + (lane >> 4) * 64 + ((lane & 3) ^ (2 * (wid & 1))) * 16;
  x.voff = lane * 16;
  x.m0Q = lds0 + wid * 4096; x.m0L = lds0 + wid * 3072; x.m0K = c.m0;
  x.srdA = mk_rsrc(in.x + (size_t)w.rb * 128 * 448, 128 * 896);
  x.srdQ = mk_rsrc(in.wq + head * 256, 448 * 8192 - head * 512);
  x.srdL = mk_rsrc(w.ws + WS_L + head * LIMG + wid * 3072, LIMG);
  x.srdG = mk_rsrc(in.gw, 896);
  PIN_V(c.tv); PIN_V(c.tb); PIN_V(c.tstep); PIN_V(c.tlim); PIN_V(x.vA[0]); PIN_V(x.vA[1]); PIN_V(x.vQ); PIN_V(c.tp0); PIN_V(c.cb); PIN_V(c.cp); PIN_V(c.cv); PIN_V(c.cstep); PIN_V(c.clim); PIN_V(c.dv); PIN_V(c.dstep); PIN_V(c.dlim);
  tile0(2); tile0(3);
  c.vA[0] += 128; c.vA[1] += 128; c.vB[0] += 0x80000u; c.vB[1] += 0x80000u;
  bf16x8 fa[8], fb[8];
  if (isv) kv_loop<true>(w, c, x, lds0, fa, fb);
  else     kv_loop<false>(w, c, x, lds0, fa, fb);
  unsigned l2 = lane; PIN_V(l2);
  const unsigned r2 = l2 & 15, g2 = l2 >> 4;
  x.srdS = mk_rsrc(w.ws + (isv ? WS_V : WS_K) + ((size_t)head * 32 + rt * 4 + wm * 2) * 32768, 65536);
  x.s1 = isv ? 1024 : 8192;
#pragma unroll
  for (int q = 0; q < 4; ++q) x.vS[q] = l2 * 16 + wn * (isv ? 8192 : 4096) + q * (isv ? 16384 : 1024);
  x.srdK = mk_rsrc(w.ws + WS_K + (size_t)head * (1u << 20), 1u << 20);
  x.ksoff[0] = (w.roleA ? 14 : 0) * 32768 + wid * 8192; x.ksoff[1] = x.ksoff[0] + 32768;
  XRd rd;
#pragma unroll
  for (int s = 0; s < 2; ++s) { rd.xa[0][s] = lds + ((wid * 16 + r2) * 128 + (((4 * s + g2) ^ ((r2 >> 1) & 7)) << 4)); rd.xa[1][s] = rd.xa[0][s] + 98304; PIN_V(rd.xa[0][s]); PIN_V(rd.xa[1][s]); }
#pragma unroll
  for (int par = 0; par < 2; ++par) { const unsigned in2 = ((l2 >> 2) & 3) * 64 + ((par ^ (g2 & 1)) * 32) + (l2 & 3) * 8;
    rd.qb[par] = lds + (28672 + g2 * 4096 + in2); rd.lb[0][par] = lds + (g2 * 1536 + in2); rd.lb[1][par] = rd.lb[0][par] + 98304;
    PIN_V(rd.qb[par]); PIN_V(rd.lb[0][par]); PIN_V(rd.lb[1][par]); }
  rd.gb = lds + (61440 + g2 * 16); PIN_V(rd.gb);
  unsigned vQe = x.vQ + 768, vAo = x.vA[1] + 7168 - 1024; PIN_V(vQe); PIN_V(vAo);
  f32x4 aq[16][2]; const LAS char* qlast[2];
#define X_QLAST() do { qlast[0] = rd.qb[0] - 28672; qlast[1] = rd.qb[1] - 28672; PIN_V(qlast[0]); PIN_V(qlast[1]); } while (0)
  u64 fl1;
  const char* f1p = sy + fl_off(head, l2 & 15, 0, F_KV);
#define XST st16_sc1
#define X_PUBLISH_KV() flag_set1(sy, fl_off(head, w.j, 0, F_KV), w.tkH, wid, x.voff)
#define X_POLL_ISSUE() asm volatile("global_load_dwordx2 %0, %1, off sc1" : "=v"(fl1) : "v"(f1p) : "memory")
#define X_POLL_CHECK() do { PIN_V(fl1); if (__builtin_amdgcn_ballot_w64(!flag_ok(fl1, w.tkH)) != 0ull) flag_wait(f1p, true, w.tkH); } while (0)
  SBAR();
  mma_acc<0>(fa[0], fb[0]); m0_set<65536>(x.m0Q); SBAR();
  mma_acc<4>(fa[1], fb[0]); const unsigned sq2_0 = s_imm<523520>(); dma_go<false, 0>(sq2_0, vQe, x.srdQ); SBAR();
  mma_acc<8>(fa[2], fb[0]); SBAR();
  mma_acc<12>(fa[3], fb[0]); SBAR();
  mma_acc<16>(fa[4], fb[0]); SBAR();
  mma_acc<20>(fa[5], fb[0]); dma_go<false, 1024>(sq2_0, x.vQ, x.srdQ); SBAR();
  mma_acc<24>(fa[6], fb[0]); const unsigned sq2_1 = s_imm<554240>(); const float d0_0 = aread1<0>(); SBAR();
  mma_acc<28>(fa[7], fb[0]); const float d0_1 = aread1<1>(); const float d0_2 = aread1<2>(); SBAR();
  mma_acc<32>(fa[0], fb[1]); const float d0_3 = aread1<3>(); const unsigned w0_0 = cvt_asm(d0_0, d0_1); SBAR();
  mma_acc<36>(fa[1], fb[1]); dma_go<false, 2048>(sq2_1, vQe, x.srdQ); const float d0_4 = aread1<4>(); SBAR();
  mma_acc<40>(fa[2], fb[1]); const float d0_5 = aread1<5>(); const unsigned w0_1 = cvt_asm(d0_2, d0_3); SBAR();
  mma_acc<44>(fa[3], fb[1]); const float d0_6 = aread1<6>(); const float d0_7 = aread1<7>(); SBAR();
  mma_acc<48>(fa[4], fb[1]); const unsigned w0_2 = cvt_asm(d0_4, d0_5); const unsigned w0_3 = cvt_asm(d0_6, d0_7); SBAR();
  mma_acc<52>(fa[5], fb[1]); const u32x4 ww0 = {w0_0, w0_1, w0_2, w0_3}; XST(ww0, x.vS[0], x.srdS, x.s1 * 0); const float d1_0 = aread1<8>(); SBAR();
  mma_acc<56>(fa[6], fb[1]); const float d1_1 = aread1<9>(); const float d1_2 = aread1<10>(); SBAR();
  mma_acc<60>(fa[7], fb[1]); const float d1_3 = aread1<11>(); const unsigned w1_0 = cvt_asm(d1_0, d1_1); KEEP(ww0); SBAR();
  mma_acc<64>(fa[0], fb[2]); const float d1_4 = aread1<12>(); const float d1_5 = aread1<13>(); SBAR();
  mma_acc<68>(fa[1], fb[2]); dma_go<false, 3072>(sq2_1, x.vQ, x.srdQ); const unsigned w1_1 = cvt_asm(d1_2, d1_3); SBAR();
  mma_acc<72>(fa[2], fb[2]); m0_set<81920>(x.m0Q); const float d1_6 = aread1<14>(); SBAR();
  mma_acc<76>(fa[3], fb[2]); const unsigned sq3_0 = s_imm<785664>(); const float d1_7 = aread1<15>(); SBAR();
  mma_acc<80>(fa[4], fb[2]); const unsigned w1_2 = cvt_asm(d1_4, d1_5); const unsigned w1_3 = cvt_asm(d1_6, d1_7); SBAR();
  mma_acc<84>(fa[5], fb[2]); const u32x4 ww1 = {w1_0, w1_1, w1_2, w1_3}; XST(ww1, x.vS[1], x.srdS, x.s1 * 0); const float d2_0 = aread1<16>(); SBAR();
  mma_acc<88>(fa[6], fb[2]); const float d2_1 = aread1<17>(); const float d2_2 = aread1<18>(); SBAR();
  mma_acc<92>(fa[7], fb[2]); const float d2_3 = aread1<19>(); const unsigned w2_0 = cvt_asm(d2_0, d2_1); KEEP(ww1); SBAR();
  mma_acc<96>(fa[0], fb[3]); const float d2_4 = aread1<20>(); const float d2_5 = aread1<21>(); SBAR();
  mma_acc<100>(fa[1], fb[3]); dma_go<false, 0>(sq3_0, vQe, x.srdQ); const unsigned w2_1 = cvt_asm(d2_2, d2_3); SBAR();
  mma_acc<104>(fa[2], fb[3]); const float d2_6 = aread1<22>(); const float d2_7 = aread1<23>(); SBAR();
  mma_acc<108>(fa[3], fb[3]); const unsigned w2_2 = cvt_asm(d2_4, d2_5); const unsigned w2_3 = cvt_asm(d2_6, d2_7); SBAR();
  mma_acc<112>(fa[4], fb[3]); const float d3_0 = aread1<24>(); const float d3_1 = aread1<25>(); SBAR();
  mma_acc<116>(fa[5], fb[3]); const u32x4 ww2 = {w2_0, w2_1, w2_2, w2_3}; XST(ww2, x.vS[2], x.srdS, x.s1 * 0); const float d3_2 = aread1<26>(); SBAR();
  mma_acc<120>(fa[6], fb[3]); const float d3_3 = aread1<27>(); const unsigned w3_0 = cvt_asm(d3_0, d3_1); SBAR();
  mma_acc<124>(fa[7], fb[3]); const float d3_4 = aread1<28>(); const float d3_5 = aread1<29>(); KEEP(ww2); SBAR();
  mma_acc<128>(fa[0], fb[4]); const unsigned w3_1 = cvt_asm(d3_2, d3_3); const float d3_6 = aread1<30>(); SBAR();
  mma_acc<132>(fa[1], fb[4]); dma_go<false, 1024>(sq3_0, x.vQ, x.srdQ); const float d3_7 = aread1<31>(); SBAR();
  mma_acc<136>(fa[2], fb[4]); const unsigned sq3_1 = s_imm<816384>(); const unsigned w3_2 = cvt_asm(d3_4, d3_5); SBAR();
  mma_acc<140>(fa[3], fb[4]); const unsigned w3_3 = cvt_asm(d3_6, d3_7); const float d4_0 = aread1<32>(); SBAR();
  mma_acc<144>(fa[4], fb[4]); const float d4_1 = aread1<33>(); const float d4_2 = aread1<34>(); SBAR();
  mma_acc<148>(fa[5], fb[4]); const u32x4 ww3 = {w3_0, w3_1, w3_2, w3_3}; XST(ww3, x.vS[3], x.srdS, x.s1 * 0); const float d4_3 = aread1<35>(); SBAR();
  mma_acc<152>(fa[6], fb[4]); const unsigned w4_0 = cvt_asm(d4_0, d4_1); const float d4_4 = aread1<36>(); SBAR();
  mma_acc<156>(fa[7], fb[4]); const float d4_5 = aread1<37>(); const unsigned w4_1 = cvt_asm(d4_2, d4_3); KEEP(ww3); SBAR();
  mma_acc<160>(fa[0], fb[5]); const float d4_6 = aread1<38>(); const float d4_7 = aread1<39>(); SBAR();
  mma_acc<164>(fa[1], fb[5]); dma_go<false, 2048>(sq3_1, vQe, x.srdQ); const unsigned w4_2 = cvt_asm(d4_4, d4_5); SBAR();
  mma_acc<168>(fa[2], fb[5]); const unsigned w4_3 = cvt_asm(d4_6, d4_7); const float d5_0 = aread1<40>(); SBAR();
  mma_acc<172>(fa[3], fb[5]); const float d5_1 = aread1<41>(); const float d5_2 = aread1<42>(); SBAR();
  mma_acc<176>(fa[4], fb[5]); const float d5_3 = aread1<43>(); const unsigned w5_0 = cvt_asm(d5_0, d5_1); SBAR();
  mma_acc<180>(fa[5], fb[5]); const u32x4 ww4 = {w4_0, w4_1, w4_2, w4_3}; XST(ww4, x.vS[0], x.srdS, x.s1 * 1); const float d5_4 = aread1<44>(); SBAR();
  mma_acc<184>(fa[6], fb[5]); const float d5_5 = aread1<45>(); const unsigned w5_1 = cvt_asm(d5_2, d5_3); SBAR();
  mma_acc<188>(fa[7], fb[5]); const float d5_6 = aread1<46>(); const float d5_7 = aread1<47>(); KEEP(ww4); SBAR();
  mma_acc<192>(fa[0], fb[6]); const unsigned w5_2 = cvt_asm(d5_4, d5_5); const unsigned w5_3 = cvt_asm(d5_6, d5_7); SBAR();
  mma_acc<196>(fa[1], fb[6]); dma_go<false, 3072>(sq3_1, x.vQ, x.srdQ); const float d6_0 = aread1<48>(); SBAR();
  mma_acc<200>(fa[2], fb[6]); const float d6_1 = aread1<49>(); const float d6_2 = aread1<50>(); SBAR();
  mma_acc<204>(fa[3], fb[6]); const bf16x8 x0_0 = ld128(rd.xa[1][0] + 32768); const float d6_3 = aread1<51>(); SBAR();
  mma_acc<208>(fa[4], fb[6]); const s16x4 l0_0_0 = ldtr(rd.lb[1][0] + 49152); const unsigned w6_0 = cvt_asm(d6_0, d6_1); SBAR();
  mma_acc<212>(fa[5], fb[6]); const u32x4 ww5 = {w5_0, w5_1, w5_2, w5_3}; XST(ww5, x.vS[1], x.srdS, x.s1 * 1); const s16x4 l0_0_1 = ldtr(rd.lb[1][0] + 49920); SBAR();
  mma_acc<216>(fa[6], fb[6]); const bf16x8 x0_1 = ld128(rd.xa[1][0] + 40960); const float d6_4 = aread1<52>(); SBAR();
  mma_acc<220>(fa[7], fb[6]); const s16x4 l0_1_0 = ldtr(rd.lb[1][1] + 49152); const float d6_5 = aread1<53>(); KEEP(ww5); SBAR();
  mma_acc<224>(fa[0], fb[7]); const s16x4 l0_1_1 = ldtr(rd.lb[1][1] + 49920); const unsigned w6_1 = cvt_asm(d6_2, d6_3); SBAR();
  mma_acc<228>(fa[1], fb[7]); const s16x4 l0_2_0 = ldtr(rd.lb[1][0] + 49408); const float d6_6 = aread1<54>(); SBAR();
  mma_acc<232>(fa[2], fb[7]); const s16x4 l0_2_1 = ldtr(rd.lb[1][0] + 50176); const float d6_7 = aread1<55>(); SBAR();
  mma_acc<236>(fa[3], fb[7]); const s16x4 l0_3_0 = ldtr(rd.lb[1][1] + 49408); const unsigned w6_2 = cvt_asm(d6_4, d6_5); SBAR();
  mma_acc<240>(fa[4], fb[7]); const s16x4 l0_3_1 = ldtr(rd.lb[1][1] + 50176); const unsigned w6_3 = cvt_asm(d6_6, d6_7); SBAR();
  mma_acc<244>(fa[5], fb[7]); const u32x4 ww6 = {w6_0, w6_1, w6_2, w6_3}; XST(ww6, x.vS[2], x.srdS, x.s1 * 1); const s16x4 l0_4_0 = ldtr(rd.lb[1][0] + 49664); SBAR();
  mma_acc<248>(fa[6], fb[7]); const s16x4 l0_4_1 = ldtr(rd.lb[1][0] + 50432); SBAR();
  mma_acc<252>(fa[7], fb[7]); const s16x4 l0_5_0 = ldtr(rd.lb[1][1] + 49664); KEEP(ww6); SBAR();
  LGKM(5); mma_acc0<0>(cat(l0_0_0, l0_0_1), x0_0); const s16x4 l0_5_1 = ldtr(rd.lb[1][1] + 50432); const float d7_0 = aread1<56>(); SBAR();
  mma_acc0<4>(cat(l0_0_0, l0_0_1), x0_1); const bf16x8 gt0 = ld128(rd.gb + 0); const float d7_1 = aread1<57>(); const float d7_2 = aread1<58>(); SBAR();
  mma_acc0<8>(cat(l0_1_0, l0_1_1), x0_0); asm volatile("s_waitcnt vmcnt(27)\n\ts_barrier" ::: ACLOB, "memory"); const s16x4 q0_0_0 = ldtr(rd.qb[0] + 0); SBAR();
  mma_acc0<12>(cat(l0_1_0, l0_1_1), x0_1); const s16x4 q0_0_1 = ldtr(rd.qb[0] + 2048); m0_set<98304>(x.m0Q); const unsigned sx2_0 = s_imm<256>(); dma_go<false, 0>(sx2_0, x.vA[0], x.srdA); SBAR();
  mma_acc0<16>(x0_0, cat(l0_2_0, l0_2_1)); const s16x4 q0_1_0 = ldtr(rd.qb[1] + 0); const float d7_3 = aread1<59>(); const unsigned w7_0 = cvt_asm(d7_0, d7_1); SBAR();
  mma_acc0<20>(x0_1, cat(l0_2_0, l0_2_1)); const s16x4 q0_1_1 = ldtr(rd.qb[1] + 2048); const float d7_4 = aread1<60>(); const float d7_5 = aread1<61>(); SBAR();
  LGKM(5); mma_acc0<24>(x0_0, cat(l0_3_0, l0_3_1)); const s16x4 q0_2_0 = ldtr(rd.qb[0] + 256); const unsigned w7_1 = cvt_asm(d7_2, d7_3); SBAR();
  mma_acc0<28>(x0_1, cat(l0_3_0, l0_3_1)); const s16x4 q0_2_1 = ldtr(rd.qb[0] + 2304); dma_go<false, 1024>(sx2_0, vAo, x.srdA); const float d7_6 = aread1<62>(); SBAR();
  mma_acc0<32>(x0_0, cat(l0_4_0, l0_4_1)); const s16x4 q0_3_0 = ldtr(rd.qb[1] + 256); const unsigned sx2_1 = s_imm<12544>(); SBAR();
  mma_acc0<36>(x0_1, cat(l0_4_0, l0_4_1)); const s16x4 q0_3_1 = ldtr(rd.qb[1] + 2304); const float d7_7 = aread1<63>(); SBAR();
  mma_acc0<40>(x0_0, cat(l0_5_0, l0_5_1)); const s16x4 q0_4_0 = ldtr(rd.qb[0] + 512); const unsigned w7_2 = cvt_asm(d7_4, d7_5); SBAR();
  mma_acc0<44>(x0_1, cat(l0_5_0, l0_5_1)); const s16x4 q0_4_1 = ldtr(rd.qb[0] + 2560); dma_go<false, 2048>(sx2_1, x.vA[0], x.srdA); SBAR();
  LGKM(5); mma_acc0<48>(gt0, x0_0); const s16x4 q0_5_0 = ldtr(rd.qb[1] + 512); SBAR();
  mma_acc0<52>(gt0, x0_1); const s16x4 q0_5_1 = ldtr(rd.qb[1] + 2560); const unsigned w7_3 = cvt_asm(d7_6, d7_7); SBAR();
  MMA_V0(aq[0][0], cat(q0_0_0, q0_0_1), x0_0); const s16x4 q0_6_0 = ldtr(rd.qb[0] + 768); const float d8_0 = aread1<64>(); SBAR();
  MMA_V0(aq[0][1], cat(q0_0_0, q0_0_1), x0_1); const s16x4 q0_6_1 = ldtr(rd.qb[0] + 2816); const u32x4 ww7 = {w7_0, w7_1, w7_2, w7_3}; XST(ww7, x.vS[3], x.srdS, x.s1 * 1); SBAR();
  MMA_V0(aq[1][0], cat(q0_1_0, q0_1_1), x0_0); const s16x4 q0_7_0 = ldtr(rd.qb[1] + 768); const float d8_1 = aread1<65>(); SBAR();
  MMA_V0(aq[1][1], cat(q0_1_0, q0_1_1), x0_1); const s16x4 q0_7_1 = ldtr(rd.qb[1] + 2816); const float d8_2 = aread1<66>(); KEEP(ww7); SBAR();
  LGKM(5); MMA_V0(aq[2][0], cat(q0_2_0, q0_2_1), x0_0); const s16x4 q0_8_0 = ldtr(rd.qb[0] + 1024); SBAR();
  MMA_V0(aq[2][1], cat(q0_2_0, q0_2_1), x0_1); const s16x4 q0_8_1 = ldtr(rd.qb[0] + 3072); dma_go<false, 3072>(sx2_1, vAo, x.srdA); SBAR();
  MMA_V0(aq[3][0], cat(q0_3_0, q0_3_1), x0_0); const s16x4 q0_9_0 = ldtr(rd.qb[1] + 1024); const s16x4 q0_9_1 = ldtr(rd.qb[1] + 3072); const float d8_3 = aread1<67>(); SBAR();
  MMA_V0(aq[3][1], cat(q0_3_0, q0_3_1), x0_1); const s16x4 q0_10_0 = ldtr(rd.qb[0] + 1280); m0_set<114688>(x.m0L); SBAR();
  MMA_V0(aq[4][0], cat(q0_4_0, q0_4_1), x0_0); const s16x4 q0_10_1 = ldtr(rd.qb[0] + 3328); const unsigned sl2 = s_imm<24576>(); const unsigned w8_0 = cvt_asm(d8_0, d8_1); SBAR();
  MMA_V0(aq[4][1], cat(q0_4_0, q0_4_1), x0_1); const s16x4 q0_11_0 = ldtr(rd.qb[1] + 1280); dma_go<true, 0>(sl2, x.voff, x.srdL); SBAR();
  LGKM(6); MMA_V0(aq[5][0], cat(q0_5_0, q0_5_1), x0_0); const s16x4 q0_11_1 = ldtr(rd.qb[1] + 3328); const float d8_4 = aread1<68>(); SBAR();
  MMA_V0(aq[5][1], cat(q0_5_0, q0_5_1), x0_1); const s16x4 q0_12_0 = ldtr(rd.qb[0] + 1536); const float d8_5 = aread1<69>(); SBAR();
  MMA_V0(aq[6][0], cat(q0_6_0, q0_6_1), x0_0); const s16x4 q0_12_1 = ldtr(rd.qb[0] + 3584); const unsigned w8_1 = cvt_asm(d8_2, d8_3); SBAR();
  MMA_V0(aq[6][1], cat(q0_6_0, q0_6_1), x0_1); const s16x4 q0_13_0 = ldtr(rd.qb[1] + 1536); dma_go<true, 1024>(sl2, x.voff, x.srdL); SBAR();
  MMA_V0(aq[7][0], cat(q0_7_0, q0_7_1), x0_0); const s16x4 q0_13_1 = ldtr(rd.qb[1] + 3584); const float d8_6 = aread1<70>(); SBAR();
  MMA_V0(aq[7][1], cat(q0_7_0, q0_7_1), x0_1); const s16x4 q0_14_0 = ldtr(rd.qb[0] + 1792); const float d8_7 = aread1<71>(); SBAR();
  LGKM(5); MMA_V0(aq[8][0], cat(q0_8_0, q0_8_1), x0_0); const s16x4 q0_14_1 = ldtr(rd.qb[0] + 3840); SBAR();
  MMA_V0(aq[8][1], cat(q0_8_0, q0_8_1), x0_1); const s16x4 q0_15_0 = ldtr(rd.qb[1] + 1792); dma_go<true, 2048>(sl2, x.voff, x.srdL); SBAR();
  MMA_V0(aq[9][0], cat(q0_9_0, q0_9_1), x0_0); const s16x4 q0_15_1 = ldtr(rd.qb[1] + 3840); const unsigned w8_2 = cvt_asm(d8_4, d8_5); SBAR();
  MMA_V0(aq[9][1], cat(q0_9_0, q0_9_1), x0_1); const bf16x8 x1_0 = ld128(rd.xa[1][1] + 32768); const unsigned w8_3 = cvt_asm(d8_6, d8_7); SBAR();
  MMA_V0(aq[10][0], cat(q0_10_0, q0_10_1), x0_0); const s16x4 l1_0_0 = ldtr(rd.lb[1][0] + 55296); const float d9_0 = aread1<72>(); SBAR();
  MMA_V0(aq[10][1], cat(q0_10_0, q0_10_1), x0_1); const s16x4 l1_0_1 = ldtr(rd.lb[1][0] + 56064); const u32x4 ww8 = {w8_0, w8_1, w8_2, w8_3}; XST(ww8, x.vS[0], x.srdS, x.s1 * 2); SBAR();
  MMA_V0(aq[11][0], cat(q0_11_0, q0_11_1), x0_0); const bf16x8 x1_1 = ld128(rd.xa[1][1] + 40960); const float d9_1 = aread1<73>(); SBAR();
  MMA_V0(aq[11][1], cat(q0_11_0, q0_11_1), x0_1); const s16x4 l1_1_0 = ldtr(rd.lb[1][1] + 55296); const float d9_2 = aread1<74>(); KEEP(ww8); SBAR();
  LGKM(5); MMA_V0(aq[12][0], cat(q0_12_0, q0_12_1), x0_0); const s16x4 l1_1_1 = ldtr(rd.lb[1][1] + 56064); SBAR();
  MMA_V0(aq[12][1], cat(q0_12_0, q0_12_1), x0_1); const s16x4 l1_2_0 = ldtr(rd.lb[1][0] + 55552); const float d9_3 = aread1<75>(); SBAR();
  MMA_V0(aq[13][0], cat(q0_13_0, q0_13_1), x0_0); const s16x4 l1_2_1 = ldtr(rd.lb[1][0] + 56320); const unsigned w9_0 = cvt_asm(d9_0, d9_1); SBAR();
  MMA_V0(aq[13][1], cat(q0_13_0, q0_13_1), x0_1); const s16x4 l1_3_0 = ldtr(rd.lb[1][1] + 55552); const float d9_4 = aread1<76>(); SBAR();
  MMA_V0(aq[14][0], cat(q0_14_0, q0_14_1), x0_0); const s16x4 l1_3_1 = ldtr(rd.lb[1][1] + 56320); const float d9_5 = aread1<77>(); SBAR();
  MMA_V0(aq[14][1], cat(q0_14_0, q0_14_1), x0_1); const s16x4 l1_4_0 = ldtr(rd.lb[1][0] + 55808); const unsigned w9_1 = cvt_asm(d9_2, d9_3); SBAR();
  MMA_V0(aq[15][0], cat(q0_15_0, q0_15_1), x0_0); const s16x4 l1_4_1 = ldtr(rd.lb[1][0] + 56576); const float d9_6 = aread1<78>(); SBAR();
  MMA_V0(aq[15][1], cat(q0_15_0, q0_15_1), x0_1); const s16x4 l1_5_0 = ldtr(rd.lb[1][1] + 55808); const float d9_7 = aread1<79>(); SBAR();
  LGKM(5); mma_acc<0>(cat(l1_0_0, l1_0_1), x1_0); const s16x4 l1_5_1 = ldtr(rd.lb[1][1] + 56576); SBAR();
  mma_acc<4>(cat(l1_0_0, l1_0_1), x1_1); const bf16x8 gt1 = ld128(rd.gb + 64); const unsigned w9_2 = cvt_asm(d9_4, d9_5); SBAR();
  mma_acc<8>(cat(l1_1_0, l1_1_1), x1_0); asm volatile("s_waitcnt vmcnt(24)\n\ts_barrier" ::: ACLOB, "memory"); const s16x4 q1_0_0 = ldtr(rd.qb[0] + 16384); SBAR();
  mma_acc<12>(cat(l1_1_0, l1_1_1), x1_1); const s16x4 q1_0_1 = ldtr(rd.qb[0] + 18432); m0_set<28672>(x.m0Q); const unsigned sq4_0 = s_imm<1047808>(); dma_go<false, 0>(sq4_0, vQe, x.srdQ); SBAR();
  mma_acc<16>(x1_0, cat(l1_2_0, l1_2_1)); const s16x4 q1_1_0 = ldtr(rd.qb[1] + 16384); const unsigned w9_3 = cvt_asm(d9_6, d9_7); SBAR();
  mma_acc<20>(x1_1, cat(l1_2_0, l1_2_1)); const s16x4 q1_1_1 = ldtr(rd.qb[1] + 18432); const float d10_0 = aread1<80>(); SBAR();
  LGKM(5); mma_acc<24>(x1_0, cat(l1_3_0, l1_3_1)); const s16x4 q1_2_0 = ldtr(rd.qb[0] + 16640); SBAR();
  mma_acc<28>(x1_1, cat(l1_3_0, l1_3_1)); const s16x4 q1_2_1 = ldtr(rd.qb[0] + 18688); const u32x4 ww9 = {w9_0, w9_1, w9_2, w9_3}; XST(ww9, x.vS[1], x.srdS, x.s1 * 2); SBAR();
  mma_acc<32>(x1_0, cat(l1_4_0, l1_4_1)); const s16x4 q1_3_0 = ldtr(rd.qb[1] + 16640); const float d10_1 = aread1<81>(); SBAR();
  mma_acc<36>(x1_1, cat(l1_4_0, l1_4_1)); const s16x4 q1_3_1 = ldtr(rd.qb[1] + 18688); const float d10_2 = aread1<82>(); KEEP(ww9); SBAR();
  mma_acc<40>(x1_0, cat(l1_5_0, l1_5_1)); const s16x4 q1_4_0 = ldtr(rd.qb[0] + 16896); const float d10_3 = aread1<83>(); SBAR();
  mma_acc<44>(x1_1, cat(l1_5_0, l1_5_1)); const s16x4 q1_4_1 = ldtr(rd.qb[0] + 18944); dma_go<false, 1024>(sq4_0, x.vQ, x.srdQ); SBAR();
  LGKM(5); mma_acc<48>(gt1, x1_0); const s16x4 q1_5_0 = ldtr(rd.qb[1] + 16896); SBAR();
  mma_acc<52>(gt1, x1_1); const s16x4 q1_5_1 = ldtr(rd.qb[1] + 18944); const unsigned sq4_1 = s_imm<1078528>(); SBAR();
  MMA_V(aq[0][0], cat(q1_0_0, q1_0_1), x1_0); const s16x4 q1_6_0 = ldtr(rd.qb[0] + 17152); const unsigned w10_0 = cvt_asm(d10_0, d10_1); SBAR();
  MMA_V(aq[0][1], cat(q1_0_0, q1_0_1), x1_1); const s16x4 q1_6_1 = ldtr(rd.qb[0] + 19200); dma_go<false, 2048>(sq4_1, vQe, x.srdQ); SBAR();
  MMA_V(aq[1][0], cat(q1_1_0, q1_1_1), x1_0); const s16x4 q1_7_0 = ldtr(rd.qb[1] + 17152); const float d10_4 = aread1<84>(); SBAR();
  MMA_V(aq[1][1], cat(q1_1_0, q1_1_1), x1_1); const s16x4 q1_7_1 = ldtr(rd.qb[1] + 19200); const float d10_5 = aread1<85>(); SBAR();
  LGKM(5); MMA_V(aq[2][0], cat(q1_2_0, q1_2_1), x1_0); const s16x4 q1_8_0 = ldtr(rd.qb[0] + 17408); SBAR();
  MMA_V(aq[2][1], cat(q1_2_0, q1_2_1), x1_1); const s16x4 q1_8_1 = ldtr(rd.qb[0] + 19456); dma_go<false, 3072>(sq4_1, x.vQ, x.srdQ); SBAR();
  MMA_V(aq[3][0], cat(q1_3_0, q1_3_1), x1_0); const s16x4 q1_9_0 = ldtr(rd.qb[1] + 17408); const s16x4 q1_9_1 = ldtr(rd.qb[1] + 19456); SBAR();
  MMA_V(aq[3][1], cat(q1_3_0, q1_3_1), x1_1); const s16x4 q1_10_0 = ldtr(rd.qb[0] + 17664); const unsigned w10_1 = cvt_asm(d10_2, d10_3); SBAR();
  MMA_V(aq[4][0], cat(q1_4_0, q1_4_1), x1_0); const s16x4 q1_10_1 = ldtr(rd.qb[0] + 19712); const float d10_6 = aread1<86>(); SBAR();
  MMA_V(aq[4][1], cat(q1_4_0, q1_4_1), x1_1); const s16x4 q1_11_0 = ldtr(rd.qb[1] + 17664); const float d10_7 = aread1<87>(); SBAR();
  LGKM(6); MMA_V(aq[5][0], cat(q1_5_0, q1_5_1), x1_0); const s16x4 q1_11_1 = ldtr(rd.qb[1] + 19712); SBAR();
  MMA_V(aq[5][1], cat(q1_5_0, q1_5_1), x1_1); const s16x4 q1_12_0 = ldtr(rd.qb[0] + 17920); const unsigned w10_2 = cvt_asm(d10_4, d10_5); SBAR();
  MMA_V(aq[6][0], cat(q1_6_0, q1_6_1), x1_0); const s16x4 q1_12_1 = ldtr(rd.qb[0] + 19968); const unsigned w10_3 = cvt_asm(d10_6, d10_7); SBAR();
  MMA_V(aq[6][1], cat(q1_6_0, q1_6_1), x1_1); const s16x4 q1_13_0 = ldtr(rd.qb[1] + 17920); const u32x4 ww10 = {w10_0, w10_1, w10_2, w10_3}; XST(ww10, x.vS[2], x.srdS, x.s1 * 2); SBAR();
  MMA_V(aq[7][0], cat(q1_7_0, q1_7_1), x1_0); const s16x4 q1_13_1 = ldtr(rd.qb[1] + 19968); const float d11_0 = aread1<88>(); SBAR();
  MMA_V(aq[7][1], cat(q1_7_0, q1_7_1), x1_1); const s16x4 q1_14_0 = ldtr(rd.qb[0] + 18176); const float d11_1 = aread1<89>(); KEEP(ww10); SBAR();
  LGKM(5); MMA_V(aq[8][0], cat(q1_8_0, q1_8_1), x1_0); const s16x4 q1_14_1 = ldtr(rd.qb[0] + 20224); SBAR();
  MMA_V(aq[8][1], cat(q1_8_0, q1_8_1), x1_1); const s16x4 q1_15_0 = ldtr(rd.qb[1] + 18176); const float d11_2 = aread1<90>(); SBAR();
  MMA_V(aq[9][0], cat(q1_9_0, q1_9_1), x1_0); const s16x4 q1_15_1 = ldtr(rd.qb[1] + 20224); const float d11_3 = aread1<91>(); SBAR();
  MMA_V(aq[9][1], cat(q1_9_0, q1_9_1), x1_1); const bf16x8 x2_0 = ld128(rd.xa[0][0] + 0); const unsigned w11_0 = cvt_asm(d11_0, d11_1); SBAR();
  MMA_V(aq[10][0], cat(q1_10_0, q1_10_1), x1_0); const s16x4 l2_0_0 = ldtr(rd.lb[0][0] + 16384); const float d11_4 = aread1<92>(); SBAR();
  MMA_V(aq[10][1], cat(q1_10_0, q1_10_1), x1_1); const s16x4 l2_0_1 = ldtr(rd.lb[0][0] + 17152); const float d11_5 = aread1<93>(); SBAR();
  MMA_V(aq[11][0], cat(q1_11_0, q1_11_1), x1_0); const bf16x8 x2_1 = ld128(rd.xa[0][0] + 8192); const unsigned w11_1 = cvt_asm(d11_2, d11_3); SBAR();
  MMA_V(aq[11][1], cat(q1_11_0, q1_11_1), x1_1); const s16x4 l2_1_0 = ldtr(rd.lb[0][1] + 16384); const float d11_6 = aread1<94>(); SBAR();
  LGKM(5); MMA_V(aq[12][0], cat(q1_12_0, q1_12_1), x1_0); const s16x4 l2_1_1 = ldtr(rd.lb[0][1] + 17152); SBAR();
  MMA_V(aq[12][1], cat(q1_12_0, q1_12_1), x1_1); const s16x4 l2_2_0 = ldtr(rd.lb[0][0] + 16640); const float d11_7 = aread1<95>(); SBAR();
  MMA_V(aq[13][0], cat(q1_13_0, q1_13_1), x1_0); const s16x4 l2_2_1 = ldtr(rd.lb[0][0] + 17408); const unsigned w11_2 = cvt_asm(d11_4, d11_5); SBAR();
  MMA_V(aq[13][1], cat(q1_13_0, q1_13_1), x1_1); const s16x4 l2_3_0 = ldtr(rd.lb[0][1] + 16640); const unsigned w11_3 = cvt_asm(d11_6, d11_7); SBAR();
  MMA_V(aq[14][0], cat(q1_14_0, q1_14_1), x1_0); const s16x4 l2_3_1 = ldtr(rd.lb[0][1] + 17408); const float d12_0 = aread1<96>(); SBAR();
  MMA_V(aq[14][1], cat(q1_14_0, q1_14_1), x1_1); const s16x4 l2_4_0 = ldtr(rd.lb[0][0] + 16896); const u32x4 ww11 = {w11_0, w11_1, w11_2, w11_3}; XST(ww11, x.vS[3], x.srdS, x.s1 * 2); SBAR();
  MMA_V(aq[15][0], cat(q1_15_0, q1_15_1), x1_0); const s16x4 l2_4_1 = ldtr(rd.lb[0][0] + 17664); const float d12_1 = aread1<97>(); SBAR();
  MMA_V(aq[15][1], cat(q1_15_0, q1_15_1), x1_1); const s16x4 l2_5_0 = ldtr(rd.lb[0][1] + 16896); const float d12_2 = aread1<98>(); KEEP(ww11); SBAR();
  LGKM(5); mma_acc<0>(cat(l2_0_0, l2_0_1), x2_0); const s16x4 l2_5_1 = ldtr(rd.lb[0][1] + 17664); SBAR();
  mma_acc<4>(cat(l2_0_0, l2_0_1), x2_1); const bf16x8 gt2 = ld128(rd.gb + 128); const float d12_3 = aread1<99>(); SBAR();
  mma_acc<8>(cat(l2_1_0, l2_1_1), x2_0); asm volatile("s_waitcnt vmcnt(26)\n\ts_barrier" ::: ACLOB, "memory"); const s16x4 q2_0_0 = ldtr(rd.qb[0] + 36864); SBAR();
  mma_acc<12>(cat(l2_1_0, l2_1_1), x2_1); const s16x4 q2_0_1 = ldtr(rd.qb[0] + 38912); m0_set<45056>(x.m0Q); const unsigned sq5_0 = s_imm<1309952>(); dma_go<false, 0>(sq5_0, vQe, x.srdQ); SBAR();
  mma_acc<16>(x2_0, cat(l2_2_0, l2_2_1)); const s16x4 q2_1_0 = ldtr(rd.qb[1] + 36864); const unsigned w12_0 = cvt_asm(d12_0, d12_1); SBAR();
  mma_acc<20>(x2_1, cat(l2_2_0, l2_2_1)); const s16x4 q2_1_1 = ldtr(rd.qb[1] + 38912); const float d12_4 = aread1<100>(); SBAR();
  LGKM(5); mma_acc<24>(x2_0, cat(l2_3_0, l2_3_1)); const s16x4 q2_2_0 = ldtr(rd.qb[0] + 37120); SBAR();
  mma_acc<28>(x2_1, cat(l2_3_0, l2_3_1)); const s16x4 q2_2_1 = ldtr(rd.qb[0] + 39168); dma_go<false, 1024>(sq5_0, x.vQ, x.srdQ); SBAR();
  mma_acc<32>(x2_0, cat(l2_4_0, l2_4_1)); const s16x4 q2_3_0 = ldtr(rd.qb[1] + 37120); const unsigned sq5_1 = s_imm<1340672>(); SBAR();
  mma_acc<36>(x2_1, cat(l2_4_0, l2_4_1)); const s16x4 q2_3_1 = ldtr(rd.qb[1] + 39168); const float d12_5 = aread1<101>(); SBAR();
  mma_acc<40>(x2_0, cat(l2_5_0, l2_5_1)); const s16x4 q2_4_0 = ldtr(rd.qb[0] + 37376); const unsigned w12_1 = cvt_asm(d12_2, d12_3); SBAR();
  mma_acc<44>(x2_1, cat(l2_5_0, l2_5_1)); const s16x4 q2_4_1 = ldtr(rd.qb[0] + 39424); dma_go<false, 2048>(sq5_1, vQe, x.srdQ); SBAR();
  LGKM(5); mma_acc<48>(gt2, x2_0); const s16x4 q2_5_0 = ldtr(rd.qb[1] + 37376); SBAR();
  mma_acc<52>(gt2, x2_1); const s16x4 q2_5_1 = ldtr(rd.qb[1] + 39424); const float d12_6 = aread1<102>(); SBAR();
  MMA_V(aq[0][0], cat(q2_0_0, q2_0_1), x2_0); const s16x4 q2_6_0 = ldtr(rd.qb[0] + 37632); const float d12_7 = aread1<103>(); SBAR();
  MMA_V(aq[0][1], cat(q2_0_0, q2_0_1), x2_1); const s16x4 q2_6_1 = ldtr(rd.qb[0] + 39680); dma_go<false, 3072>(sq5_1, x.vQ, x.srdQ); SBAR();
  MMA_V(aq[1][0], cat(q2_1_0, q2_1_1), x2_0); const s16x4 q2_7_0 = ldtr(rd.qb[1] + 37632); m0_set<131072>(x.m0Q); SBAR();
  MMA_V(aq[1][1], cat(q2_1_0, q2_1_1), x2_1); const s16x4 q2_7_1 = ldtr(rd.qb[1] + 39680); const unsigned sx3_0 = s_imm<384>(); SBAR();
  LGKM(5); MMA_V(aq[2][0], cat(q2_2_0, q2_2_1), x2_0); const s16x4 q2_8_0 = ldtr(rd.qb[0] + 37888); SBAR();
  MMA_V(aq[2][1], cat(q2_2_0, q2_2_1), x2_1); const s16x4 q2_8_1 = ldtr(rd.qb[0] + 39936); dma_go<false, 0>(sx3_0, x.vA[0], x.srdA); SBAR();
  MMA_V(aq[3][0], cat(q2_3_0, q2_3_1), x2_0); const s16x4 q2_9_0 = ldtr(rd.qb[1] + 37888); const s16x4 q2_9_1 = ldtr(rd.qb[1] + 39936); SBAR();
  MMA_V(aq[3][1], cat(q2_3_0, q2_3_1), x2_1); const s16x4 q2_10_0 = ldtr(rd.qb[0] + 38144); const unsigned w12_2 = cvt_asm(d12_4, d12_5); SBAR();
  MMA_V(aq[4][0], cat(q2_4_0, q2_4_1), x2_0); const s16x4 q2_10_1 = ldtr(rd.qb[0] + 40192); const unsigned w12_3 = cvt_asm(d12_6, d12_7); SBAR();
  MMA_V(aq[4][1], cat(q2_4_0, q2_4_1), x2_1); const s16x4 q2_11_0 = ldtr(rd.qb[1] + 38144); const u32x4 ww12 = {w12_0, w12_1, w12_2, w12_3}; XST(ww12, x.vS[0], x.srdS, x.s1 * 3); SBAR();
  LGKM(6); MMA_V(aq[5][0], cat(q2_5_0, q2_5_1), x2_0); const s16x4 q2_11_1 = ldtr(rd.qb[1] + 40192); SBAR();
  MMA_V(aq[5][1], cat(q2_5_0, q2_5_1), x2_1); const s16x4 q2_12_0 = ldtr(rd.qb[0] + 38400); const float d13_0 = aread1<104>(); KEEP(ww12); SBAR();
  MMA_V(aq[6][0], cat(q2_6_0, q2_6_1), x2_0); const s16x4 q2_12_1 = ldtr(rd.qb[0] + 40448); const float d13_1 = aread1<105>(); SBAR();
  MMA_V(aq[6][1], cat(q2_6_0, q2_6_1), x2_1); const s16x4 q2_13_0 = ldtr(rd.qb[1] + 38400); dma_go<false, 1024>(sx3_0, vAo, x.srdA); SBAR();
  MMA_V(aq[7][0], cat(q2_7_0, q2_7_1), x2_0); const s16x4 q2_13_1 = ldtr(rd.qb[1] + 40448); const unsigned sx3_1 = s_imm<12672>(); SBAR();
  MMA_V(aq[7][1], cat(q2_7_0, q2_7_1), x2_1); const s16x4 q2_14_0 = ldtr(rd.qb[0] + 38656); const float d13_2 = aread1<106>(); SBAR();
  LGKM(5); MMA_V(aq[8][0], cat(q2_8_0, q2_8_1), x2_0); const s16x4 q2_14_1 = ldtr(rd.qb[0] + 40704); SBAR();
  MMA_V(aq[8][1], cat(q2_8_0, q2_8_1), x2_1); const s16x4 q2_15_0 = ldtr(rd.qb[1] + 38656); dma_go<false, 2048>(sx3_1, x.vA[0], x.srdA); SBAR();
  MMA_V(aq[9][0], cat(q2_9_0, q2_9_1), x2_0); const s16x4 q2_15_1 = ldtr(rd.qb[1] + 40704); const float d13_3 = aread1<107>(); SBAR();
  MMA_V(aq[9][1], cat(q2_9_0, q2_9_1), x2_1); const bf16x8 x3_0 = ld128(rd.xa[0][1] + 0); const unsigned w13_0 = cvt_asm(d13_0, d13_1); SBAR();
  MMA_V(aq[10][0], cat(q2_10_0, q2_10_1), x2_0); const s16x4 l3_0_0 = ldtr(rd.lb[0][0] + 22528); const float d13_4 = aread1<108>(); SBAR();
  MMA_V(aq[10][1], cat(q2_10_0, q2_10_1), x2_1); const s16x4 l3_0_1 = ldtr(rd.lb[0][0] + 23296); dma_go<false, 3072>(sx3_1, vAo, x.srdA); SBAR();
  MMA_V(aq[11][0], cat(q2_11_0, q2_11_1), x2_0); const bf16x8 x3_1 = ld128(rd.xa[0][1] + 8192); const float d13_5 = aread1<109>(); SBAR();
  MMA_V(aq[11][1], cat(q2_11_0, q2_11_1), x2_1); const s16x4 l3_1_0 = ldtr(rd.lb[0][1] + 22528); const unsigned w13_1 = cvt_asm(d13_2, d13_3); SBAR();
  LGKM(5); MMA_V(aq[12][0], cat(q2_12_0, q2_12_1), x2_0); const s16x4 l3_1_1 = ldtr(rd.lb[0][1] + 23296); SBAR();
  MMA_V(aq[12][1], cat(q2_12_0, q2_12_1), x2_1); const s16x4 l3_2_0 = ldtr(rd.lb[0][0] + 22784); const float d13_6 = aread1<110>(); SBAR();
  MMA_V(aq[13][0], cat(q2_13_0, q2_13_1), x2_0); const s16x4 l3_2_1 = ldtr(rd.lb[0][0] + 23552); const float d13_7 = aread1<111>(); SBAR();
  MMA_V(aq[13][1], cat(q2_13_0, q2_13_1), x2_1); const s16x4 l3_3_0 = ldtr(rd.lb[0][1] + 22784); const unsigned w13_2 = cvt_asm(d13_4, d13_5); SBAR();
  MMA_V(aq[14][0], cat(q2_14_0, q2_14_1), x2_0); const s16x4 l3_3_1 = ldtr(rd.lb[0][1] + 23552); const unsigned w13_3 = cvt_asm(d13_6, d13_7); SBAR();
  MMA_V(aq[14][1], cat(q2_14_0, q2_14_1), x2_1); const s16x4 l3_4_0 = ldtr(rd.lb[0][0] + 23040); const u32x4 ww13 = {w13_0, w13_1, w13_2, w13_3}; XST(ww13, x.vS[1], x.srdS, x.s1 * 3); SBAR();
  MMA_V(aq[15][0], cat(q2_15_0, q2_15_1), x2_0); const s16x4 l3_4_1 = ldtr(rd.lb[0][0] + 23808); const float d14_0 = aread1<112>(); SBAR();
  MMA_V(aq[15][1], cat(q2_15_0, q2_15_1), x2_1); const s16x4 l3_5_0 = ldtr(rd.lb[0][1] + 23040); const float d14_1 = aread1<113>(); KEEP(ww13); SBAR();
  LGKM(5); mma_acc<0>(cat(l3_0_0, l3_0_1), x3_0); const s16x4 l3_5_1 = ldtr(rd.lb[0][1] + 23808); SBAR();
  mma_acc<4>(cat(l3_0_0, l3_0_1), x3_1); const bf16x8 gt3 = ld128(rd.gb + 192); const float d14_2 = aread1<114>(); SBAR();
  mma_acc<8>(cat(l3_1_0, l3_1_1), x3_0); asm volatile("s_waitcnt vmcnt(18)\n\ts_barrier" ::: ACLOB, "memory"); const s16x4 q3_0_0 = ldtr(rd.qb[0] + 53248); SBAR();
  mma_acc<12>(cat(l3_1_0, l3_1_1), x3_1); const s16x4 q3_0_1 = ldtr(rd.qb[0] + 55296); m0_set<65536>(x.m0Q); const unsigned sq6_0 = s_imm<1572096>(); dma_go<false, 0>(sq6_0, vQe, x.srdQ); SBAR();
  mma_acc<16>(x3_0, cat(l3_2_0, l3_2_1)); const s16x4 q3_1_0 = ldtr(rd.qb[1] + 53248); const float d14_3 = aread1<115>(); SBAR();
  mma_acc<20>(x3_1, cat(l3_2_0, l3_2_1)); const s16x4 q3_1_1 = ldtr(rd.qb[1] + 55296); const unsigned w14_0 = cvt_asm(d14_0, d14_1); SBAR();
  LGKM(5); mma_acc<24>(x3_0, cat(l3_3_0, l3_3_1)); const s16x4 q3_2_0 = ldtr(rd.qb[0] + 53504); SBAR();
  mma_acc<28>(x3_1, cat(l3_3_0, l3_3_1)); const s16x4 q3_2_1 = ldtr(rd.qb[0] + 55552); dma_go<false, 1024>(sq6_0, x.vQ, x.srdQ); SBAR();
  mma_acc<32>(x3_0, cat(l3_4_0, l3_4_1)); const s16x4 q3_3_0 = ldtr(rd.qb[1] + 53504); const unsigned sq6_1 = s_imm<1602816>(); SBAR();
  mma_acc<36>(x3_1, cat(l3_4_0, l3_4_1)); const s16x4 q3_3_1 = ldtr(rd.qb[1] + 55552); const float d14_4 = aread1<116>(); SBAR();
  mma_acc<40>(x3_0, cat(l3_5_0, l3_5_1)); const s16x4 q3_4_0 = ldtr(rd.qb[0] + 53760); const float d14_5 = aread1<117>(); SBAR();
  mma_acc<44>(x3_1, cat(l3_5_0, l3_5_1)); const s16x4 q3_4_1 = ldtr(rd.qb[0] + 55808); dma_go<false, 2048>(sq6_1, vQe, x.srdQ); SBAR();
  LGKM(5); mma_acc<48>(gt3, x3_0); const s16x4 q3_5_0 = ldtr(rd.qb[1] + 53760); SBAR();
  mma_acc<52>(gt3, x3_1); const s16x4 q3_5_1 = ldtr(rd.qb[1] + 55808); const unsigned w14_1 = cvt_asm(d14_2, d14_3); SBAR();
  MMA_V(aq[0][0], cat(q3_0_0, q3_0_1), x3_0); const s16x4 q3_6_0 = ldtr(rd.qb[0] + 54016); const float d14_6 = aread1<118>(); SBAR();
  MMA_V(aq[0][1], cat(q3_0_0, q3_0_1), x3_1); const s16x4 q3_6_1 = ldtr(rd.qb[0] + 56064); dma_go<false, 3072>(sq6_1, x.vQ, x.srdQ); SBAR();
  MMA_V(aq[1][0], cat(q3_1_0, q3_1_1), x3_0); const s16x4 q3_7_0 = ldtr(rd.qb[1] + 54016); m0_set<147456>(x.m0L); SBAR();
  MMA_V(aq[1][1], cat(q3_1_0, q3_1_1), x3_1); const s16x4 q3_7_1 = ldtr(rd.qb[1] + 56064); const unsigned sl3 = s_imm<36864>(); SBAR();
  LGKM(5); MMA_V(aq[2][0], cat(q3_2_0, q3_2_1), x3_0); const s16x4 q3_8_0 = ldtr(rd.qb[0] + 54272); SBAR();
  MMA_V(aq[2][1], cat(q3_2_0, q3_2_1), x3_1); const s16x4 q3_8_1 = ldtr(rd.qb[0] + 56320); dma_go<true, 0>(sl3, x.voff, x.srdL); SBAR();
  MMA_V(aq[3][0], cat(q3_3_0, q3_3_1), x3_0); const s16x4 q3_9_0 = ldtr(rd.qb[1] + 54272); const s16x4 q3_9_1 = ldtr(rd.qb[1] + 56320); SBAR();
  MMA_V(aq[3][1], cat(q3_3_0, q3_3_1), x3_1); const s16x4 q3_10_0 = ldtr(rd.qb[0] + 54528); const float d14_7 = aread1<119>(); SBAR();
  MMA_V(aq[4][0], cat(q3_4_0, q3_4_1), x3_0); const s16x4 q3_10_1 = ldtr(rd.qb[0] + 56576); const unsigned w14_2 = cvt_asm(d14_4, d14_5); SBAR();
  MMA_V(aq[4][1], cat(q3_4_0, q3_4_1), x3_1); const s16x4 q3_11_0 = ldtr(rd.qb[1] + 54528); dma_go<true, 1024>(sl3, x.voff, x.srdL); SBAR();
  LGKM(6); MMA_V(aq[5][0], cat(q3_5_0, q3_5_1), x3_0); const s16x4 q3_11_1 = ldtr(rd.qb[1] + 56576); SBAR();
  MMA_V(aq[5][1], cat(q3_5_0, q3_5_1), x3_1); const s16x4 q3_12_0 = ldtr(rd.qb[0] + 54784); const unsigned w14_3 = cvt_asm(d14_6, d14_7); SBAR();
  MMA_V(aq[6][0], cat(q3_6_0, q3_6_1), x3_0); const s16x4 q3_12_1 = ldtr(rd.qb[0] + 56832); const float d15_0 = aread1<120>(); SBAR();
  MMA_V(aq[6][1], cat(q3_6_0, q3_6_1), x3_1); const s16x4 q3_13_0 = ldtr(rd.qb[1] + 54784); const u32x4 ww14 = {w14_0, w14_1, w14_2, w14_3}; XST(ww14, x.vS[2], x.srdS, x.s1 * 3); SBAR();
  MMA_V(aq[7][0], cat(q3_7_0, q3_7_1), x3_0); const s16x4 q3_13_1 = ldtr(rd.qb[1] + 56832); const float d15_1 = aread1<121>(); SBAR();
  MMA_V(aq[7][1], cat(q3_7_0, q3_7_1), x3_1); const s16x4 q3_14_0 = ldtr(rd.qb[0] + 55040); const float d15_2 = aread1<122>(); KEEP(ww14); SBAR();
  LGKM(5); MMA_V(aq[8][0], cat(q3_8_0, q3_8_1), x3_0); const s16x4 q3_14_1 = ldtr(rd.qb[0] + 57088); SBAR();
  MMA_V(aq[8][1], cat(q3_8_0, q3_8_1), x3_1); const s16x4 q3_15_0 = ldtr(rd.qb[1] + 55040); dma_go<true, 2048>(sl3, x.voff, x.srdL); SBAR();
  MMA_V(aq[9][0], cat(q3_9_0, q3_9_1), x3_0); const s16x4 q3_15_1 = ldtr(rd.qb[1] + 57088); const float d15_3 = aread1<123>(); SBAR();
  MMA_V(aq[9][1], cat(q3_9_0, q3_9_1), x3_1); const bf16x8 x4_0 = ld128(rd.xa[1][0] + 0); const unsigned w15_0 = cvt_asm(d15_0, d15_1); SBAR();
  MMA_V(aq[10][0], cat(q3_10_0, q3_10_1), x3_0); const s16x4 l4_0_0 = ldtr(rd.lb[1][0] + 16384); const float d15_4 = aread1<124>(); SBAR();
  MMA_V(aq[10][1], cat(q3_10_0, q3_10_1), x3_1); const s16x4 l4_0_1 = ldtr(rd.lb[1][0] + 17152); const float d15_5 = aread1<125>(); SBAR();
  MMA_V(aq[11][0], cat(q3_11_0, q3_11_1), x3_0); const bf16x8 x4_1 = ld128(rd.xa[1][0] + 8192); const unsigned w15_1 = cvt_asm(d15_2, d15_3); SBAR();
  MMA_V(aq[11][1], cat(q3_11_0, q3_11_1), x3_1); const s16x4 l4_1_0 = ldtr(rd.lb[1][1] + 16384); const float d15_6 = aread1<126>(); SBAR();
  LGKM(5); MMA_V(aq[12][0], cat(q3_12_0, q3_12_1), x3_0); const s16x4 l4_1_1 = ldtr(rd.lb[1][1] + 17152); SBAR();
  MMA_V(aq[12][1], cat(q3_12_0, q3_12_1), x3_1); const s16x4 l4_2_0 = ldtr(rd.lb[1][0] + 16640); const float d15_7 = aread1<127>(); SBAR();
  MMA_V(aq[13][0], cat(q3_13_0, q3_13_1), x3_0); const s16x4 l4_2_1 = ldtr(rd.lb[1][0] + 17408); const unsigned w15_2 = cvt_asm(d15_4, d15_5); SBAR();
  MMA_V(aq[13][1], cat(q3_13_0, q3_13_1), x3_1); const s16x4 l4_3_0 = ldtr(rd.lb[1][1] + 16640); const unsigned w15_3 = cvt_asm(d15_6, d15_7); SBAR();
  MMA_V(aq[14][0], cat(q3_14_0, q3_14_1), x3_0); const s16x4 l4_3_1 = ldtr(rd.lb[1][1] + 17408); const float d16_0 = aread1<128>(); SBAR();
  MMA_V(aq[14][1], cat(q3_14_0, q3_14_1), x3_1); const s16x4 l4_4_0 = ldtr(rd.lb[1][0] + 16896); const u32x4 ww15 = {w15_0, w15_1, w15_2, w15_3}; XST(ww15, x.vS[3], x.srdS, x.s1 * 3); SBAR();
  MMA_V(aq[15][0], cat(q3_15_0, q3_15_1), x3_0); const s16x4 l4_4_1 = ldtr(rd.lb[1][0] + 17664); const float d16_1 = aread1<129>(); SBAR();
  MMA_V(aq[15][1], cat(q3_15_0, q3_15_1), x3_1); const s16x4 l4_5_0 = ldtr(rd.lb[1][1] + 16896); const float d16_2 = aread1<130>(); KEEP(ww15); SBAR();
  LGKM(5); mma_acc<0>(cat(l4_0_0, l4_0_1), x4_0); const s16x4 l4_5_1 = ldtr(rd.lb[1][1] + 17664); SBAR();
  mma_acc<4>(cat(l4_0_0, l4_0_1), x4_1); const bf16x8 gt4 = ld128(rd.gb + 256); const float d16_3 = aread1<131>(); SBAR();
  mma_acc<8>(cat(l4_1_0, l4_1_1), x4_0); asm volatile("s_waitcnt vmcnt(21)\n\ts_barrier" ::: ACLOB, "memory");
  const s16x4 q4_0_0 = ldtr(rd.qb[0] + 0); SBAR();
  mma_acc<12>(cat(l4_1_0, l4_1_1), x4_1); const s16x4 q4_0_1 = ldtr(rd.qb[0] + 2048); m0_set<81920>(x.m0Q); const unsigned sq7_0 = s_imm<1834240>(); dma_go<false, 0>(sq7_0, vQe, x.srdQ); SBAR();
  mma_acc<16>(x4_0, cat(l4_2_0, l4_2_1)); const s16x4 q4_1_0 = ldtr(rd.qb[1] + 0); const unsigned w16_0 = cvt_asm(d16_0, d16_1); SBAR();
  mma_acc<20>(x4_1, cat(l4_2_0, l4_2_1)); const s16x4 q4_1_1 = ldtr(rd.qb[1] + 2048); const float d16_4 = aread1<132>(); SBAR();
  LGKM(5); mma_acc<24>(x4_0, cat(l4_3_0, l4_3_1)); const s16x4 q4_2_0 = ldtr(rd.qb[0] + 256); SBAR();
  mma_acc<28>(x4_1, cat(l4_3_0, l4_3_1)); const s16x4 q4_2_1 = ldtr(rd.qb[0] + 2304); dma_go<false, 1024>(sq7_0, x.vQ, x.srdQ); SBAR();
  mma_acc<32>(x4_0, cat(l4_4_0, l4_4_1)); const s16x4 q4_3_0 = ldtr(rd.qb[1] + 256); const unsigned sq7_1 = s_imm<1864960>(); SBAR();
  mma_acc<36>(x4_1, cat(l4_4_0, l4_4_1)); const s16x4 q4_3_1 = ldtr(rd.qb[1] + 2304); const float d16_5 = aread1<133>(); SBAR();
  mma_acc<40>(x4_0, cat(l4_5_0, l4_5_1)); const s16x4 q4_4_0 = ldtr(rd.qb[0] + 512); const unsigned w16_1 = cvt_asm(d16_2, d16_3); SBAR();
  mma_acc<44>(x4_1, cat(l4_5_0, l4_5_1)); const s16x4 q4_4_1 = ldtr(rd.qb[0] + 2560); dma_go<false, 2048>(sq7_1, vQe, x.srdQ); SBAR();
  LGKM(5); mma_acc<48>(gt4, x4_0); const s16x4 q4_5_0 = ldtr(rd.qb[1] + 512); SBAR();
  mma_acc<52>(gt4, x4_1); const s16x4 q4_5_1 = ldtr(rd.qb[1] + 2560); const float d16_6 = aread1<134>(); SBAR();
  MMA_V(aq[0][0], cat(q4_0_0, q4_0_1), x4_0); const s16x4 q4_6_0 = ldtr(rd.qb[0] + 768); const float d16_7 = aread1<135>(); SBAR();
  MMA_V(aq[0][1], cat(q4_0_0, q4_0_1), x4_1); const s16x4 q4_6_1 = ldtr(rd.qb[0] + 2816); dma_go<false, 3072>(sq7_1, x.vQ, x.srdQ); SBAR();
  MMA_V(aq[1][0], cat(q4_1_0, q4_1_1), x4_0); const s16x4 q4_7_0 = ldtr(rd.qb[1] + 768); m0_set<0>(x.m0Q); SBAR();
  MMA_V(aq[1][1], cat(q4_1_0, q4_1_1), x4_1); const s16x4 q4_7_1 = ldtr(rd.qb[1] + 2816); const unsigned sx4_0 = s_imm<512>(); SBAR();
  LGKM(5); MMA_V(aq[2][0], cat(q4_2_0, q4_2_1), x4_0); const s16x4 q4_8_0 = ldtr(rd.qb[0] + 1024); SBAR();
  MMA_V(aq[2][1], cat(q4_2_0, q4_2_1), x4_1); const s16x4 q4_8_1 = ldtr(rd.qb[0] + 3072); dma_go<false, 0>(sx4_0, x.vA[0], x.srdA); SBAR();
  MMA_V(aq[3][0], cat(q4_3_0, q4_3_1), x4_0); const s16x4 q4_9_0 = ldtr(rd.qb[1] + 1024); const s16x4 q4_9_1 = ldtr(rd.qb[1] + 3072); SBAR();
  MMA_V(aq[3][1], cat(q4_3_0, q4_3_1), x4_1); const s16x4 q4_10_0 = ldtr(rd.qb[0] + 1280); const unsigned w16_2 = cvt_asm(d16_4, d16_5); SBAR();
  MMA_V(aq[4][0], cat(q4_4_0, q4_4_1), x4_0); const s16x4 q4_10_1 = ldtr(rd.qb[0] + 3328); const unsigned w16_3 = cvt_asm(d16_6, d16_7); SBAR();
  MMA_V(aq[4][1], cat(q4_4_0, q4_4_1), x4_1); const s16x4 q4_11_0 = ldtr(rd.qb[1] + 1280); const u32x4 ww16 = {w16_0, w16_1, w16_2, w16_3}; XST(ww16, x.vS[0], x.srdS, x.s1 * 4); SBAR();
  LGKM(6); MMA_V(aq[5][0], cat(q4_5_0, q4_5_1), x4_0); const s16x4 q4_11_1 = ldtr(rd.qb[1] + 3328); SBAR();
  MMA_V(aq[5][1], cat(q4_5_0, q4_5_1), x4_1); const s16x4 q4_12_0 = ldtr(rd.qb[0] + 1536); const float d17_0 = aread1<136>(); KEEP(ww16); SBAR();
  MMA_V(aq[6][0], cat(q4_6_0, q4_6_1), x4_0); const s16x4 q4_12_1 = ldtr(rd.qb[0] + 3584); const float d17_1 = aread1<137>(); SBAR();
  MMA_V(aq[6][1], cat(q4_6_0, q4_6_1), x4_1); const s16x4 q4_13_0 = ldtr(rd.qb[1] + 1536); dma_go<false, 1024>(sx4_0, vAo, x.srdA); SBAR();
  MMA_V(aq[7][0], cat(q4_7_0, q4_7_1), x4_0); const s16x4 q4_13_1 = ldtr(rd.qb[1] + 3584); const unsigned sx4_1 = s_imm<12800>(); SBAR();
  MMA_V(aq[7][1], cat(q4_7_0, q4_7_1), x4_1); const s16x4 q4_14_0 = ldtr(rd.qb[0] + 1792); const float d17_2 = aread1<138>(); SBAR();
  LGKM(5); MMA_V(aq[8][0], cat(q4_8_0, q4_8_1), x4_0); const s16x4 q4_14_1 = ldtr(rd.qb[0] + 3840); SBAR();
  MMA_V(aq[8][1], cat(q4_8_0, q4_8_1), x4_1); const s16x4 q4_15_0 = ldtr(rd.qb[1] + 1792); dma_go<false, 2048>(sx4_1, x.vA[0], x.srdA); SBAR();
  MMA_V(aq[9][0], cat(q4_9_0, q4_9_1), x4_0); const s16x4 q4_15_1 = ldtr(rd.qb[1] + 3840); const float d17_3 = aread1<139>(); SBAR();
  MMA_V(aq[9][1], cat(q4_9_0, q4_9_1), x4_1); const bf16x8 x5_0 = ld128(rd.xa[1][1] + 0); const unsigned w17_0 = cvt_asm(d17_0, d17_1); SBAR();
  MMA_V(aq[10][0], cat(q4_10_0, q4_10_1), x4_0); const s16x4 l5_0_0 = ldtr(rd.lb[1][0] + 22528); const float d17_4 = aread1<140>(); SBAR();
  MMA_V(aq[10][1], cat(q4_10_0, q4_10_1), x4_1); const s16x4 l5_0_1 = ldtr(rd.lb[1][0] + 23296); dma_go<false, 3072>(sx4_1, vAo, x.srdA); SBAR();
  MMA_V(aq[11][0], cat(q4_11_0, q4_11_1), x4_0); const bf16x8 x5_1 = ld128(rd.xa[1][1] + 8192); const float d17_5 = aread1<141>(); SBAR();
  MMA_V(aq[11][1], cat(q4_11_0, q4_11_1), x4_1); const s16x4 l5_1_0 = ldtr(rd.lb[1][1] + 22528); const unsigned w17_1 = cvt_asm(d17_2, d17_3); SBAR();
  LGKM(5); MMA_V(aq[12][0], cat(q4_12_0, q4_12_1), x4_0); const s16x4 l5_1_1 = ldtr(rd.lb[1][1] + 23296); SBAR();
  MMA_V(aq[12][1], cat(q4_12_0, q4_12_1), x4_1); const s16x4 l5_2_0 = ldtr(rd.lb[1][0] + 22784); const float d17_6 = aread1<142>(); SBAR();
  MMA_V(aq[13][0], cat(q4_13_0, q4_13_1), x4_0); const s16x4 l5_2_1 = ldtr(rd.lb[1][0] + 23552); const float d17_7 = aread1<143>(); SBAR();
  MMA_V(aq[13][1], cat(q4_13_0, q4_13_1), x4_1); const s16x4 l5_3_0 = ldtr(rd.lb[1][1] + 22784); const unsigned w17_2 = cvt_asm(d17_4, d17_5); SBAR();
  MMA_V(aq[14][0], cat(q4_14_0, q4_14_1), x4_0); const s16x4 l5_3_1 = ldtr(rd.lb[1][1] + 23552); const unsigned w17_3 = cvt_asm(d17_6, d17_7); SBAR();
  MMA_V(aq[14][1], cat(q4_14_0, q4_14_1), x4_1); const s16x4 l5_4_0 = ldtr(rd.lb[1][0] + 23040); const u32x4 ww17 = {w17_0, w17_1, w17_2, w17_3}; XST(ww17, x.vS[1], x.srdS, x.s1 * 4); SBAR();
  MMA_V(aq[15][0], cat(q4_15_0, q4_15_1), x4_0); const s16x4 l5_4_1 = ldtr(rd.lb[1][0] + 23808); const float d18_0 = aread1<144>(); SBAR();
  MMA_V(aq[15][1], cat(q4_15_0, q4_15_1), x4_1); const s16x4 l5_5_0 = ldtr(rd.lb[1][1] + 23040); const float d18_1 = aread1<145>(); KEEP(ww17); SBAR();
  LGKM(5); mma_acc<0>(cat(l5_0_0, l5_0_1), x5_0); const s16x4 l5_5_1 = ldtr(rd.lb[1][1] + 23808); SBAR();
  mma_acc<4>(cat(l5_0_0, l5_0_1), x5_1); const bf16x8 gt5 = ld128(rd.gb + 320); const float d18_2 = aread1<146>(); SBAR();
  mma_acc<8>(cat(l5_1_0, l5_1_1), x5_0); asm volatile("s_waitcnt vmcnt(11)\n\ts_barrier" ::: ACLOB, "memory"); const s16x4 q5_0_0 = ldtr(rd.qb[0] + 16384); SBAR();
  mma_acc<12>(cat(l5_1_0, l5_1_1), x5_1); const s16x4 q5_0_1 = ldtr(rd.qb[0] + 18432); m0_set<28672>(x.m0Q); const unsigned sq8_0 = s_imm<2096384>(); dma_go<false, 0>(sq8_0, vQe, x.srdQ); SBAR();
  mma_acc<16>(x5_0, cat(l5_2_0, l5_2_1)); const s16x4 q5_1_0 = ldtr(rd.qb[1] + 16384); const float d18_3 = aread1<147>(); SBAR();
  mma_acc<20>(x5_1, cat(l5_2_0, l5_2_1)); const s16x4 q5_1_1 = ldtr(rd.qb[1] + 18432); const unsigned w18_0 = cvt_asm(d18_0, d18_1); SBAR();
  LGKM(5); mma_acc<24>(x5_0, cat(l5_3_0, l5_3_1)); const s16x4 q5_2_0 = ldtr(rd.qb[0] + 16640); SBAR();
  mma_acc<28>(x5_1, cat(l5_3_0, l5_3_1)); const s16x4 q5_2_1 = ldtr(rd.qb[0] + 18688); dma_go<false, 1024>(sq8_0, x.vQ, x.srdQ); SBAR();
  mma_acc<32>(x5_0, cat(l5_4_0, l5_4_1)); const s16x4 q5_3_0 = ldtr(rd.qb[1] + 16640); const unsigned sq8_1 = s_imm<2127104>(); SBAR();
  mma_acc<36>(x5_1, cat(l5_4_0, l5_4_1)); const s16x4 q5_3_1 = ldtr(rd.qb[1] + 18688); const float d18_4 = aread1<148>(); SBAR();
  mma_acc<40>(x5_0, cat(l5_5_0, l5_5_1)); const s16x4 q5_4_0 = ldtr(rd.qb[0] + 16896); const float d18_5 = aread1<149>(); SBAR();
  mma_acc<44>(x5_1, cat(l5_5_0, l5_5_1)); const s16x4 q5_4_1 = ldtr(rd.qb[0] + 18944); dma_go<false, 2048>(sq8_1, vQe, x.srdQ); SBAR();
  LGKM(5); mma_acc<48>(gt5, x5_0); const s16x4 q5_5_0 = ldtr(rd.qb[1] + 16896); SBAR();
  mma_acc<52>(gt5, x5_1); const s16x4 q5_5_1 = ldtr(rd.qb[1] + 18944); const unsigned w18_1 = cvt_asm(d18_2, d18_3); SBAR();
  MMA_V(aq[0][0], cat(q5_0_0, q5_0_1), x5_0); const s16x4 q5_6_0 = ldtr(rd.qb[0] + 17152); const float d18_6 = aread1<150>(); SBAR();
  MMA_V(aq[0][1], cat(q5_0_0, q5_0_1), x5_1); const s16x4 q5_6_1 = ldtr(rd.qb[0] + 19200); dma_go<false, 3072>(sq8_1, x.vQ, x.srdQ); SBAR();
  MMA_V(aq[1][0], cat(q5_1_0, q5_1_1), x5_0); const s16x4 q5_7_0 = ldtr(rd.qb[1] + 17152); m0_set<16384>(x.m0L); SBAR();
  MMA_V(aq[1][1], cat(q5_1_0, q5_1_1), x5_1); const s16x4 q5_7_1 = ldtr(rd.qb[1] + 19200); const unsigned sl4 = s_imm<49152>(); SBAR();
  LGKM(5); MMA_V(aq[2][0], cat(q5_2_0, q5_2_1), x5_0); const s16x4 q5_8_0 = ldtr(rd.qb[0] + 17408); SBAR();
  MMA_V(aq[2][1], cat(q5_2_0, q5_2_1), x5_1); const s16x4 q5_8_1 = ldtr(rd.qb[0] + 19456); dma_go<true, 0>(sl4, x.voff, x.srdL); SBAR();
  MMA_V(aq[3][0], cat(q5_3_0, q5_3_1), x5_0); const s16x4 q5_9_0 = ldtr(rd.qb[1] + 17408); const s16x4 q5_9_1 = ldtr(rd.qb[1] + 19456); SBAR();
  MMA_V(aq[3][1], cat(q5_3_0, q5_3_1), x5_1); const s16x4 q5_10_0 = ldtr(rd.qb[0] + 17664); const float d18_7 = aread1<151>(); SBAR();
  MMA_V(aq[4][0], cat(q5_4_0, q5_4_1), x5_0); const s16x4 q5_10_1 = ldtr(rd.qb[0] + 19712); const unsigned w18_2 = cvt_asm(d18_4, d18_5); SBAR();
  MMA_V(aq[4][1], cat(q5_4_0, q5_4_1), x5_1); const s16x4 q5_11_0 = ldtr(rd.qb[1] + 17664); dma_go<true, 1024>(sl4, x.voff, x.srdL); SBAR();
  LGKM(6); MMA_V(aq[5][0], cat(q5_5_0, q5_5_1), x5_0); const s16x4 q5_11_1 = ldtr(rd.qb[1] + 19712); SBAR();
  MMA_V(aq[5][1], cat(q5_5_0, q5_5_1), x5_1); const s16x4 q5_12_0 = ldtr(rd.qb[0] + 17920); const unsigned w18_3 = cvt_asm(d18_6, d18_7); SBAR();
  MMA_V(aq[6][0], cat(q5_6_0, q5_6_1), x5_0); const s16x4 q5_12_1 = ldtr(rd.qb[0] + 19968); const float d19_0 = aread1<152>(); SBAR();
  MMA_V(aq[6][1], cat(q5_6_0, q5_6_1), x5_1); const s16x4 q5_13_0 = ldtr(rd.qb[1] + 17920); const u32x4 ww18 = {w18_0, w18_1, w18_2, w18_3}; XST(ww18, x.vS[2], x.srdS, x.s1 * 4); SBAR();
  MMA_V(aq[7][0], cat(q5_7_0, q5_7_1), x5_0); const s16x4 q5_13_1 = ldtr(rd.qb[1] + 19968); const float d19_1 = aread1<153>(); SBAR();
  MMA_V(aq[7][1], cat(q5_7_0, q5_7_1), x5_1); const s16x4 q5_14_0 = ldtr(rd.qb[0] + 18176); const float d19_2 = aread1<154>(); KEEP(ww18); SBAR();
  LGKM(5); MMA_V(aq[8][0], cat(q5_8_0, q5_8_1), x5_0); const s16x4 q5_14_1 = ldtr(rd.qb[0] + 20224); SBAR();
  MMA_V(aq[8][1], cat(q5_8_0, q5_8_1), x5_1); const s16x4 q5_15_0 = ldtr(rd.qb[1] + 18176); dma_go<true, 2048>(sl4, x.voff, x.srdL); SBAR();
  MMA_V(aq[9][0], cat(q5_9_0, q5_9_1), x5_0); const s16x4 q5_15_1 = ldtr(rd.qb[1] + 20224); const float d19_3 = aread1<155>(); SBAR();
  MMA_V(aq[9][1], cat(q5_9_0, q5_9_1), x5_1); const bf16x8 x6_0 = ld128(rd.xa[1][0] + 32768); const unsigned w19_0 = cvt_asm(d19_0, d19_1); SBAR();
  MMA_V(aq[10][0], cat(q5_10_0, q5_10_1), x5_0); const s16x4 l6_0_0 = ldtr(rd.lb[1][0] + 49152); const float d19_4 = aread1<156>(); SBAR();
  MMA_V(aq[10][1], cat(q5_10_0, q5_10_1), x5_1); const s16x4 l6_0_1 = ldtr(rd.lb[1][0] + 49920); const float d19_5 = aread1<157>(); SBAR();
  MMA_V(aq[11][0], cat(q5_11_0, q5_11_1), x5_0); const bf16x8 x6_1 = ld128(rd.xa[1][0] + 40960); const unsigned w19_1 = cvt_asm(d19_2, d19_3); SBAR();
  MMA_V(aq[11][1], cat(q5_11_0, q5_11_1), x5_1); const s16x4 l6_1_0 = ldtr(rd.lb[1][1] + 49152); const float d19_6 = aread1<158>(); SBAR();
  LGKM(5); MMA_V(aq[12][0], cat(q5_12_0, q5_12_1), x5_0); const s16x4 l6_1_1 = ldtr(rd.lb[1][1] + 49920); SBAR();
  MMA_V(aq[12][1], cat(q5_12_0, q5_12_1), x5_1); const s16x4 l6_2_0 = ldtr(rd.lb[1][0] + 49408); const float d19_7 = aread1<159>(); SBAR();
  MMA_V(aq[13][0], cat(q5_13_0, q5_13_1), x5_0); const s16x4 l6_2_1 = ldtr(rd.lb[1][0] + 50176); const unsigned w19_2 = cvt_asm(d19_4, d19_5); SBAR();
  MMA_V(aq[13][1], cat(q5_13_0, q5_13_1), x5_1); const s16x4 l6_3_0 = ldtr(rd.lb[1][1] + 49408); const unsigned w19_3 = cvt_asm(d19_6, d19_7); SBAR();
  MMA_V(aq[14][0], cat(q5_14_0, q5_14_1), x5_0); const s16x4 l6_3_1 = ldtr(rd.lb[1][1] + 50176); const float d20_0 = aread1<160>(); SBAR();
  MMA_V(aq[14][1], cat(q5_14_0, q5_14_1), x5_1); const s16x4 l6_4_0 = ldtr(rd.lb[1][0] + 49664); const u32x4 ww19 = {w19_0, w19_1, w19_2, w19_3}; XST(ww19, x.vS[3], x.srdS, x.s1 * 4); SBAR();
  MMA_V(aq[15][0], cat(q5_15_0, q5_15_1), x5_0); const s16x4 l6_4_1 = ldtr(rd.lb[1][0] + 50432); const float d20_1 = aread1<161>(); SBAR();
  MMA_V(aq[15][1], cat(q5_15_0, q5_15_1), x5_1); const s16x4 l6_5_0 = ldtr(rd.lb[1][1] + 49664); const float d20_2 = aread1<162>(); KEEP(ww19); SBAR();
  LGKM(5); mma_acc<0>(cat(l6_0_0, l6_0_1), x6_0); const s16x4 l6_5_1 = ldtr(rd.lb[1][1] + 50432); SBAR();
  mma_acc<4>(cat(l6_0_0, l6_0_1), x6_1); const bf16x8 gt6 = ld128(rd.gb + 384); const float d20_3 = aread1<163>(); SBAR();
  mma_acc<8>(cat(l6_1_0, l6_1_1), x6_0); asm volatile("s_waitcnt vmcnt(20)\n\ts_barrier" ::: ACLOB, "memory"); const s16x4 q6_0_0 = ldtr(rd.qb[0] + 36864); SBAR();
  mma_acc<12>(cat(l6_1_0, l6_1_1), x6_1); const s16x4 q6_0_1 = ldtr(rd.qb[0] + 38912); m0_set<45056>(x.m0Q); const unsigned sq9_0 = s_imm<2358528>(); dma_go<false, 0>(sq9_0, vQe, x.srdQ); SBAR();
  mma_acc<16>(x6_0, cat(l6_2_0, l6_2_1)); const s16x4 q6_1_0 = ldtr(rd.qb[1] + 36864); const unsigned w20_0 = cvt_asm(d20_0, d20_1); SBAR();
  mma_acc<20>(x6_1, cat(l6_2_0, l6_2_1)); const s16x4 q6_1_1 = ldtr(rd.qb[1] + 38912); const float d20_4 = aread1<164>(); SBAR();
  LGKM(5); mma_acc<24>(x6_0, cat(l6_3_0, l6_3_1)); const s16x4 q6_2_0 = ldtr(rd.qb[0] + 37120); SBAR();
  mma_acc<28>(x6_1, cat(l6_3_0, l6_3_1)); const s16x4 q6_2_1 = ldtr(rd.qb[0] + 39168); dma_go<false, 1024>(sq9_0, x.vQ, x.srdQ); SBAR();
  mma_acc<32>(x6_0, cat(l6_4_0, l6_4_1)); const s16x4 q6_3_0 = ldtr(rd.qb[1] + 37120); const unsigned sq9_1 = s_imm<2389248>(); SBAR();
  mma_acc<36>(x6_1, cat(l6_4_0, l6_4_1)); const s16x4 q6_3_1 = ldtr(rd.qb[1] + 39168); const float d20_5 = aread1<165>(); SBAR();
  mma_acc<40>(x6_0, cat(l6_5_0, l6_5_1)); const s16x4 q6_4_0 = ldtr(rd.qb[0] + 37376); const unsigned w20_1 = cvt_asm(d20_2, d20_3); SBAR();
  mma_acc<44>(x6_1, cat(l6_5_0, l6_5_1)); const s16x4 q6_4_1 = ldtr(rd.qb[0] + 39424); dma_go<false, 2048>(sq9_1, vQe, x.srdQ); SBAR();
  LGKM(5); mma_acc<48>(gt6, x6_0); const s16x4 q6_5_0 = ldtr(rd.qb[1] + 37376); SBAR();
  mma_acc<52>(gt6, x6_1); const s16x4 q6_5_1 = ldtr(rd.qb[1] + 39424); const float d20_6 = aread1<166>(); SBAR();
  MMA_V(aq[0][0], cat(q6_0_0, q6_0_1), x6_0); const s16x4 q6_6_0 = ldtr(rd.qb[0] + 37632); const float d20_7 = aread1<167>(); SBAR();
  MMA_V(aq[0][1], cat(q6_0_0, q6_0_1), x6_1); const s16x4 q6_6_1 = ldtr(rd.qb[0] + 39680); dma_go<false, 3072>(sq9_1, x.vQ, x.srdQ); SBAR();
  MMA_V(aq[1][0], cat(q6_1_0, q6_1_1), x6_0); const s16x4 q6_7_0 = ldtr(rd.qb[1] + 37632); m0_set<98304>(x.m0Q); SBAR();
  MMA_V(aq[1][1], cat(q6_1_0, q6_1_1), x6_1); const s16x4 q6_7_1 = ldtr(rd.qb[1] + 39680); const unsigned sx5_0 = s_imm<640>(); SBAR();
  LGKM(5); MMA_V(aq[2][0], cat(q6_2_0, q6_2_1), x6_0); const s16x4 q6_8_0 = ldtr(rd.qb[0] + 37888); SBAR();
  MMA_V(aq[2][1], cat(q6_2_0, q6_2_1), x6_1); const s16x4 q6_8_1 = ldtr(rd.qb[0] + 39936); dma_go<false, 0>(sx5_0, x.vA[0], x.srdA); SBAR();
  MMA_V(aq[3][0], cat(q6_3_0, q6_3_1), x6_0); const s16x4 q6_9_0 = ldtr(rd.qb[1] + 37888); const s16x4 q6_9_1 = ldtr(rd.qb[1] + 39936); SBAR();
  MMA_V(aq[3][1], cat(q6_3_0, q6_3_1), x6_1); const s16x4 q6_10_0 = ldtr(rd.qb[0] + 38144); const unsigned w20_2 = cvt_asm(d20_4, d20_5); SBAR();
  MMA_V(aq[4][0], cat(q6_4_0, q6_4_1), x6_0); const s16x4 q6_10_1 = ldtr(rd.qb[0] + 40192); const unsigned w20_3 = cvt_asm(d20_6, d20_7); SBAR();
  MMA_V(aq[4][1], cat(q6_4_0, q6_4_1), x6_1); const s16x4 q6_11_0 = ldtr(rd.qb[1] + 38144); const u32x4 ww20 = {w20_0, w20_1, w20_2, w20_3}; XST(ww20, x.vS[0], x.srdS, x.s1 * 5); SBAR();
  LGKM(6); MMA_V(aq[5][0], cat(q6_5_0, q6_5_1), x6_0); const s16x4 q6_11_1 = ldtr(rd.qb[1] + 40192); SBAR();
  MMA_V(aq[5][1], cat(q6_5_0, q6_5_1), x6_1); const s16x4 q6_12_0 = ldtr(rd.qb[0] + 38400); const float d21_0 = aread1<168>(); KEEP(ww20); SBAR();
  MMA_V(aq[6][0], cat(q6_6_0, q6_6_1), x6_0); const s16x4 q6_12_1 = ldtr(rd.qb[0] + 40448); const float d21_1 = aread1<169>(); SBAR();
  MMA_V(aq[6][1], cat(q6_6_0, q6_6_1), x6_1); const s16x4 q6_13_0 = ldtr(rd.qb[1] + 38400); dma_go<false, 1024>(sx5_0, vAo, x.srdA); SBAR();
  MMA_V(aq[7][0], cat(q6_7_0, q6_7_1), x6_0); const s16x4 q6_13_1 = ldtr(rd.qb[1] + 40448); const unsigned sx5_1 = s_imm<12928>(); SBAR();
  MMA_V(aq[7][1], cat(q6_7_0, q6_7_1), x6_1); const s16x4 q6_14_0 = ldtr(rd.qb[0] + 38656); const float d21_2 = aread1<170>(); SBAR();
  LGKM(5); MMA_V(aq[8][0], cat(q6_8_0, q6_8_1), x6_0); const s16x4 q6_14_1 = ldtr(rd.qb[0] + 40704); SBAR();
  MMA_V(aq[8][1], cat(q6_8_0, q6_8_1), x6_1); const s16x4 q6_15_0 = ldtr(rd.qb[1] + 38656); dma_go<false, 2048>(sx5_1, x.vA[0], x.srdA); const float d21_3 = aread1<171>(); SBAR();
  MMA_V(aq[9][0], cat(q6_9_0, q6_9_1), x6_0); const s16x4 q6_15_1 = ldtr(rd.qb[1] + 40704); const unsigned w21_0 = cvt_asm(d21_0, d21_1); SBAR();
  MMA_V(aq[9][1], cat(q6_9_0, q6_9_1), x6_1); const bf16x8 x7_0 = ld128(rd.xa[1][1] + 32768); const float d21_4 = aread1<172>(); SBAR();
  MMA_V(aq[10][0], cat(q6_10_0, q6_10_1), x6_0); const s16x4 l7_0_0 = ldtr(rd.lb[1][0] + 55296); const float d21_5 = aread1<173>(); SBAR();
  MMA_V(aq[10][1], cat(q6_10_0, q6_10_1), x6_1); const s16x4 l7_0_1 = ldtr(rd.lb[1][0] + 56064); dma_go<false, 3072>(sx5_1, vAo, x.srdA); SBAR();
  MMA_V(aq[11][0], cat(q6_11_0, q6_11_1), x6_0); const bf16x8 x7_1 = ld128(rd.xa[1][1] + 40960); const unsigned w21_1 = cvt_asm(d21_2, d21_3); SBAR();
  MMA_V(aq[11][1], cat(q6_11_0, q6_11_1), x6_1); const s16x4 l7_1_0 = ldtr(rd.lb[1][1] + 55296); const float d21_6 = aread1<174>(); SBAR();
  LGKM(5); MMA_V(aq[12][0], cat(q6_12_0, q6_12_1), x6_0); const s16x4 l7_1_1 = ldtr(rd.lb[1][1] + 56064); SBAR();
  MMA_V(aq[12][1], cat(q6_12_0, q6_12_1), x6_1); const s16x4 l7_2_0 = ldtr(rd.lb[1][0] + 55552); const float d21_7 = aread1<175>(); SBAR();
  MMA_V(aq[13][0], cat(q6_13_0, q6_13_1), x6_0); const s16x4 l7_2_1 = ldtr(rd.lb[1][0] + 56320); const unsigned w21_2 = cvt_asm(d21_4, d21_5); SBAR();
  MMA_V(aq[13][1], cat(q6_13_0, q6_13_1), x6_1); const s16x4 l7_3_0 = ldtr(rd.lb[1][1] + 55552); const unsigned w21_3 = cvt_asm(d21_6, d21_7); SBAR();
  MMA_V(aq[14][0], cat(q6_14_0, q6_14_1), x6_0); const s16x4 l7_3_1 = ldtr(rd.lb[1][1] + 56320); const float d22_0 = aread1<176>(); SBAR();
  MMA_V(aq[14][1], cat(q6_14_0, q6_14_1), x6_1); const s16x4 l7_4_0 = ldtr(rd.lb[1][0] + 55808); const u32x4 ww21 = {w21_0, w21_1, w21_2, w21_3}; XST(ww21, x.vS[1], x.srdS, x.s1 * 5); SBAR();
  MMA_V(aq[15][0], cat(q6_15_0, q6_15_1), x6_0); const s16x4 l7_4_1 = ldtr(rd.lb[1][0] + 56576); const float d22_1 = aread1<177>(); SBAR();
  MMA_V(aq[15][1], cat(q6_15_0, q6_15_1), x6_1); const s16x4 l7_5_0 = ldtr(rd.lb[1][1] + 55808); const float d22_2 = aread1<178>(); KEEP(ww21); SBAR();
  LGKM(5); mma_acc<0>(cat(l7_0_0, l7_0_1), x7_0); const s16x4 l7_5_1 = ldtr(rd.lb[1][1] + 56576); SBAR();
  mma_acc<4>(cat(l7_0_0, l7_0_1), x7_1); const bf16x8 gt7 = ld128(rd.gb + 448); const float d22_3 = aread1<179>(); SBAR();
  mma_acc<8>(cat(l7_1_0, l7_1_1), x7_0); asm volatile("s_waitcnt vmcnt(11)\n\ts_barrier" ::: ACLOB, "memory"); const s16x4 q7_0_0 = ldtr(rd.qb[0] + 53248); SBAR();
  mma_acc<12>(cat(l7_1_0, l7_1_1), x7_1); const s16x4 q7_0_1 = ldtr(rd.qb[0] + 55296); m0_set<65536>(x.m0Q); const unsigned sq10_0 = s_imm<2620672>(); dma_go<false, 0>(sq10_0, vQe, x.srdQ); SBAR();
  mma_acc<16>(x7_0, cat(l7_2_0, l7_2_1)); const s16x4 q7_1_0 = ldtr(rd.qb[1] + 53248); const unsigned w22_0 = cvt_asm(d22_0, d22_1); SBAR();
  mma_acc<20>(x7_1, cat(l7_2_0, l7_2_1)); const s16x4 q7_1_1 = ldtr(rd.qb[1] + 55296); const float d22_4 = aread1<180>(); SBAR();
  LGKM(5); mma_acc<24>(x7_0, cat(l7_3_0, l7_3_1)); const s16x4 q7_2_0 = ldtr(rd.qb[0] + 53504); SBAR();
  mma_acc<28>(x7_1, cat(l7_3_0, l7_3_1)); const s16x4 q7_2_1 = ldtr(rd.qb[0] + 55552); dma_go<false, 1024>(sq10_0, x.vQ, x.srdQ); SBAR();
  mma_acc<32>(x7_0, cat(l7_4_0, l7_4_1)); const s16x4 q7_3_0 = ldtr(rd.qb[1] + 53504); const unsigned sq10_1 = s_imm<2651392>(); SBAR();
  mma_acc<36>(x7_1, cat(l7_4_0, l7_4_1)); const s16x4 q7_3_1 = ldtr(rd.qb[1] + 55552); const float d22_5 = aread1<181>(); SBAR();
  mma_acc<40>(x7_0, cat(l7_5_0, l7_5_1)); const s16x4 q7_4_0 = ldtr(rd.qb[0] + 53760); const unsigned w22_1 = cvt_asm(d22_2, d22_3); SBAR();
  mma_acc<44>(x7_1, cat(l7_5_0, l7_5_1)); const s16x4 q7_4_1 = ldtr(rd.qb[0] + 55808); dma_go<false, 2048>(sq10_1, vQe, x.srdQ); SBAR();
  LGKM(5); mma_acc<48>(gt7, x7_0); const s16x4 q7_5_0 = ldtr(rd.qb[1] + 53760); SBAR();
  mma_acc<52>(gt7, x7_1); const s16x4 q7_5_1 = ldtr(rd.qb[1] + 55808); const float d22_6 = aread1<182>(); SBAR();
  MMA_V(aq[0][0], cat(q7_0_0, q7_0_1), x7_0); const s16x4 q7_6_0 = ldtr(rd.qb[0] + 54016); const float d22_7 = aread1<183>(); SBAR();
  MMA_V(aq[0][1], cat(q7_0_0, q7_0_1), x7_1); const s16x4 q7_6_1 = ldtr(rd.qb[0] + 56064); dma_go<false, 3072>(sq10_1, x.vQ, x.srdQ); SBAR();
  MMA_V(aq[1][0], cat(q7_1_0, q7_1_1), x7_0); const s16x4 q7_7_0 = ldtr(rd.qb[1] + 54016); m0_set<114688>(x.m0L); SBAR();
  MMA_V(aq[1][1], cat(q7_1_0, q7_1_1), x7_1); const s16x4 q7_7_1 = ldtr(rd.qb[1] + 56064); const unsigned sl5 = s_imm<61440>(); SBAR();
  LGKM(5); MMA_V(aq[2][0], cat(q7_2_0, q7_2_1), x7_0); const s16x4 q7_8_0 = ldtr(rd.qb[0] + 54272); SBAR();
  MMA_V(aq[2][1], cat(q7_2_0, q7_2_1), x7_1); const s16x4 q7_8_1 = ldtr(rd.qb[0] + 56320); dma_go<true, 0>(sl5, x.voff, x.srdL); SBAR();
  MMA_V(aq[3][0], cat(q7_3_0, q7_3_1), x7_0); const s16x4 q7_9_0 = ldtr(rd.qb[1] + 54272); const s16x4 q7_9_1 = ldtr(rd.qb[1] + 56320); const unsigned w22_2 = cvt_asm(d22_4, d22_5); SBAR();
  MMA_V(aq[3][1], cat(q7_3_0, q7_3_1), x7_1); const s16x4 q7_10_0 = ldtr(rd.qb[0] + 54528); const unsigned w22_3 = cvt_asm(d22_6, d22_7); SBAR();
  MMA_V(aq[4][0], cat(q7_4_0, q7_4_1), x7_0); const s16x4 q7_10_1 = ldtr(rd.qb[0] + 56576); const float d23_0 = aread1<184>(); SBAR();
  MMA_V(aq[4][1], cat(q7_4_0, q7_4_1), x7_1); const s16x4 q7_11_0 = ldtr(rd.qb[1] + 54528); const u32x4 ww22 = {w22_0, w22_1, w22_2, w22_3}; XST(ww22, x.vS[2], x.srdS, x.s1 * 5); SBAR();
  LGKM(6); MMA_V(aq[5][0], cat(q7_5_0, q7_5_1), x7_0); const s16x4 q7_11_1 = ldtr(rd.qb[1] + 56576); SBAR();
  MMA_V(aq[5][1], cat(q7_5_0, q7_5_1), x7_1); const s16x4 q7_12_0 = ldtr(rd.qb[0] + 54784); const float d23_1 = aread1<185>(); KEEP(ww22); SBAR();
  MMA_V(aq[6][0], cat(q7_6_0, q7_6_1), x7_0); const s16x4 q7_12_1 = ldtr(rd.qb[0] + 56832); const float d23_2 = aread1<186>(); SBAR();
  MMA_V(aq[6][1], cat(q7_6_0, q7_6_1), x7_1); const s16x4 q7_13_0 = ldtr(rd.qb[1] + 54784); dma_go<true, 1024>(sl5, x.voff, x.srdL); SBAR();
  MMA_V(aq[7][0], cat(q7_7_0, q7_7_1), x7_0); const s16x4 q7_13_1 = ldtr(rd.qb[1] + 56832); const float d23_3 = aread1<187>(); SBAR();
  MMA_V(aq[7][1], cat(q7_7_0, q7_7_1), x7_1); const s16x4 q7_14_0 = ldtr(rd.qb[0] + 55040); const unsigned w23_0 = cvt_asm(d23_0, d23_1); SBAR();
  LGKM(5); MMA_V(aq[8][0], cat(q7_8_0, q7_8_1), x7_0); const s16x4 q7_14_1 = ldtr(rd.qb[0] + 57088); SBAR();
  MMA_V(aq[8][1], cat(q7_8_0, q7_8_1), x7_1); const s16x4 q7_15_0 = ldtr(rd.qb[1] + 55040); dma_go<true, 2048>(sl5, x.voff, x.srdL); SBAR();
  MMA_V(aq[9][0], cat(q7_9_0, q7_9_1), x7_0); const s16x4 q7_15_1 = ldtr(rd.qb[1] + 57088); const float d23_4 = aread1<188>(); SBAR();
  MMA_V(aq[9][1], cat(q7_9_0, q7_9_1), x7_1); const bf16x8 x8_0 = ld128(rd.xa[0][0] + 0); const float d23_5 = aread1<189>(); SBAR();
  MMA_V(aq[10][0], cat(q7_10_0, q7_10_1), x7_0); const s16x4 l8_0_0 = ldtr(rd.lb[0][0] + 16384); const unsigned w23_1 = cvt_asm(d23_2, d23_3); SBAR();
  MMA_V(aq[10][1], cat(q7_10_0, q7_10_1), x7_1); const s16x4 l8_0_1 = ldtr(rd.lb[0][0] + 17152); const float d23_6 = aread1<190>(); SBAR();
  MMA_V(aq[11][0], cat(q7_11_0, q7_11_1), x7_0); const bf16x8 x8_1 = ld128(rd.xa[0][0] + 8192); const float d23_7 = aread1<191>(); SBAR();
  MMA_V(aq[11][1], cat(q7_11_0, q7_11_1), x7_1); const s16x4 l8_1_0 = ldtr(rd.lb[0][1] + 16384); const unsigned w23_2 = cvt_asm(d23_4, d23_5); SBAR();
  LGKM(5); MMA_V(aq[12][0], cat(q7_12_0, q7_12_1), x7_0); const s16x4 l8_1_1 = ldtr(rd.lb[0][1] + 17152); SBAR();
  MMA_V(aq[12][1], cat(q7_12_0, q7_12_1), x7_1); const s16x4 l8_2_0 = ldtr(rd.lb[0][0] + 16640); const unsigned w23_3 = cvt_asm(d23_6, d23_7); SBAR();
  MMA_V(aq[13][0], cat(q7_13_0, q7_13_1), x7_0); const s16x4 l8_2_1 = ldtr(rd.lb[0][0] + 17408); const float d24_0 = aread1<192>(); SBAR();
  MMA_V(aq[13][1], cat(q7_13_0, q7_13_1), x7_1); const s16x4 l8_3_0 = ldtr(rd.lb[0][1] + 16640); const float d24_1 = aread1<193>(); SBAR();
  MMA_V(aq[14][0], cat(q7_14_0, q7_14_1), x7_0); const s16x4 l8_3_1 = ldtr(rd.lb[0][1] + 17408); const float d24_2 = aread1<194>(); SBAR();
  MMA_V(aq[14][1], cat(q7_14_0, q7_14_1), x7_1); const s16x4 l8_4_0 = ldtr(rd.lb[0][0] + 16896); const u32x4 ww23 = {w23_0, w23_1, w23_2, w23_3}; XST(ww23, x.vS[3], x.srdS, x.s1 * 5); SBAR();
  MMA_V(aq[15][0], cat(q7_15_0, q7_15_1), x7_0); const s16x4 l8_4_1 = ldtr(rd.lb[0][0] + 17664); const float d24_3 = aread1<195>(); SBAR();
  MMA_V(aq[15][1], cat(q7_15_0, q7_15_1), x7_1); const s16x4 l8_5_0 = ldtr(rd.lb[0][1] + 16896); const unsigned w24_0 = cvt_asm(d24_0, d24_1); KEEP(ww23); SBAR();
  LGKM(5); mma_acc<0>(cat(l8_0_0, l8_0_1), x8_0); const s16x4 l8_5_1 = ldtr(rd.lb[0][1] + 17664); SBAR();
  mma_acc<4>(cat(l8_0_0, l8_0_1), x8_1); const bf16x8 gt8 = ld128(rd.gb + 512); const float d24_4 = aread1<196>(); SBAR();
  mma_acc<8>(cat(l8_1_0, l8_1_1), x8_0); asm volatile("s_waitcnt vmcnt(20)\n\ts_barrier" ::: ACLOB, "memory");
  const s16x4 q8_0_0 = ldtr(rd.qb[0] + 0); SBAR();
  mma_acc<12>(cat(l8_1_0, l8_1_1), x8_1); const s16x4 q8_0_1 = ldtr(rd.qb[0] + 2048); m0_set<81920>(x.m0Q); const unsigned sq11_0 = s_imm<2882816>(); dma_go<false, 0>(sq11_0, vQe, x.srdQ); SBAR();
  mma_acc<16>(x8_0, cat(l8_2_0, l8_2_1)); const s16x4 q8_1_0 = ldtr(rd.qb[1] + 0); const float d24_5 = aread1<197>(); SBAR();
  mma_acc<20>(x8_1, cat(l8_2_0, l8_2_1)); const s16x4 q8_1_1 = ldtr(rd.qb[1] + 2048); const unsigned w24_1 = cvt_asm(d24_2, d24_3); SBAR();
  LGKM(5); mma_acc<24>(x8_0, cat(l8_3_0, l8_3_1)); const s16x4 q8_2_0 = ldtr(rd.qb[0] + 256); SBAR();
  mma_acc<28>(x8_1, cat(l8_3_0, l8_3_1)); const s16x4 q8_2_1 = ldtr(rd.qb[0] + 2304); dma_go<false, 1024>(sq11_0, x.vQ, x.srdQ); SBAR();
  mma_acc<32>(x8_0, cat(l8_4_0, l8_4_1)); const s16x4 q8_3_0 = ldtr(rd.qb[1] + 256); const unsigned sq11_1 = s_imm<2913536>(); SBAR();
  mma_acc<36>(x8_1, cat(l8_4_0, l8_4_1)); const s16x4 q8_3_1 = ldtr(rd.qb[1] + 2304); const float d24_6 = aread1<198>(); SBAR();
  mma_acc<40>(x8_0, cat(l8_5_0, l8_5_1)); const s16x4 q8_4_0 = ldtr(rd.qb[0] + 512); const float d24_7 = aread1<199>(); SBAR();
  mma_acc<44>(x8_1, cat(l8_5_0, l8_5_1)); const s16x4 q8_4_1 = ldtr(rd.qb[0] + 2560); dma_go<false, 2048>(sq11_1, vQe, x.srdQ); SBAR();
  LGKM(5); mma_acc<48>(gt8, x8_0); const s16x4 q8_5_0 = ldtr(rd.qb[1] + 512); SBAR();
  mma_acc<52>(gt8, x8_1); const s16x4 q8_5_1 = ldtr(rd.qb[1] + 2560); const unsigned w24_2 = cvt_asm(d24_4, d24_5); SBAR();
  MMA_V(aq[0][0], cat(q8_0_0, q8_0_1), x8_0); const s16x4 q8_6_0 = ldtr(rd.qb[0] + 768); const unsigned w24_3 = cvt_asm(d24_6, d24_7); SBAR();
  MMA_V(aq[0][1], cat(q8_0_0, q8_0_1), x8_1); const s16x4 q8_6_1 = ldtr(rd.qb[0] + 2816); const u32x4 ww24 = {w24_0, w24_1, w24_2, w24_3}; XST(ww24, x.vS[0], x.srdS, x.s1 * 6); SBAR();
  MMA_V(aq[1][0], cat(q8_1_0, q8_1_1), x8_0); const s16x4 q8_7_0 = ldtr(rd.qb[1] + 768); const float d25_0 = aread1<200>(); SBAR();
  MMA_V(aq[1][1], cat(q8_1_0, q8_1_1), x8_1); const s16x4 q8_7_1 = ldtr(rd.qb[1] + 2816); const float d25_1 = aread1<201>(); KEEP(ww24); SBAR();
  LGKM(5); MMA_V(aq[2][0], cat(q8_2_0, q8_2_1), x8_0); const s16x4 q8_8_0 = ldtr(rd.qb[0] + 1024); SBAR();
  MMA_V(aq[2][1], cat(q8_2_0, q8_2_1), x8_1); const s16x4 q8_8_1 = ldtr(rd.qb[0] + 3072); dma_go<false, 3072>(sq11_1, x.vQ, x.srdQ); SBAR();
  MMA_V(aq[3][0], cat(q8_3_0, q8_3_1), x8_0); const s16x4 q8_9_0 = ldtr(rd.qb[1] + 1024); const s16x4 q8_9_1 = ldtr(rd.qb[1] + 3072); SBAR();
  MMA_V(aq[3][1], cat(q8_3_0, q8_3_1), x8_1); const s16x4 q8_10_0 = ldtr(rd.qb[0] + 1280); m0_set<131072>(x.m0Q); SBAR();
  MMA_V(aq[4][0], cat(q8_4_0, q8_4_1), x8_0); const s16x4 q8_10_1 = ldtr(rd.qb[0] + 3328); const unsigned sx6_0 = s_imm<768>(); SBAR();
  MMA_V(aq[4][1], cat(q8_4_0, q8_4_1), x8_1); const s16x4 q8_11_0 = ldtr(rd.qb[1] + 1280); dma_go<false, 0>(sx6_0, x.vA[0], x.srdA); SBAR();
  LGKM(6); MMA_V(aq[5][0], cat(q8_5_0, q8_5_1), x8_0); const s16x4 q8_11_1 = ldtr(rd.qb[1] + 3328); SBAR();
  MMA_V(aq[5][1], cat(q8_5_0, q8_5_1), x8_1); const s16x4 q8_12_0 = ldtr(rd.qb[0] + 1536); const float d25_2 = aread1<202>(); SBAR();
  MMA_V(aq[6][0], cat(q8_6_0, q8_6_1), x8_0); const s16x4 q8_12_1 = ldtr(rd.qb[0] + 3584); const float d25_3 = aread1<203>(); SBAR();
  MMA_V(aq[6][1], cat(q8_6_0, q8_6_1), x8_1); const s16x4 q8_13_0 = ldtr(rd.qb[1] + 1536); dma_go<false, 1024>(sx6_0, vAo, x.srdA); SBAR();
  MMA_V(aq[7][0], cat(q8_7_0, q8_7_1), x8_0); const s16x4 q8_13_1 = ldtr(rd.qb[1] + 3584); const unsigned sx6_1 = s_imm<13056>(); SBAR();
  MMA_V(aq[7][1], cat(q8_7_0, q8_7_1), x8_1); const s16x4 q8_14_0 = ldtr(rd.qb[0] + 1792); const unsigned w25_0 = cvt_asm(d25_0, d25_1); SBAR();
  LGKM(5); MMA_V(aq[8][0], cat(q8_8_0, q8_8_1), x8_0); const s16x4 q8_14_1 = ldtr(rd.qb[0] + 3840); SBAR();
  MMA_V(aq[8][1], cat(q8_8_0, q8_8_1), x8_1); const s16x4 q8_15_0 = ldtr(rd.qb[1] + 1792); dma_go<false, 2048>(sx6_1, x.vA[0], x.srdA); const float d25_4 = aread1<204>(); SBAR();
  MMA_V(aq[9][0], cat(q8_9_0, q8_9_1), x8_0); const s16x4 q8_15_1 = ldtr(rd.qb[1] + 3840); const float d25_5 = aread1<205>(); SBAR();
  MMA_V(aq[9][1], cat(q8_9_0, q8_9_1), x8_1); const bf16x8 x9_0 = ld128(rd.xa[0][1] + 0); const unsigned w25_1 = cvt_asm(d25_2, d25_3); SBAR();
  MMA_V(aq[10][0], cat(q8_10_0, q8_10_1), x8_0); const s16x4 l9_0_0 = ldtr(rd.lb[0][0] + 22528); const float d25_6 = aread1<206>(); SBAR();
  MMA_V(aq[10][1], cat(q8_10_0, q8_10_1), x8_1); const s16x4 l9_0_1 = ldtr(rd.lb[0][0] + 23296); dma_go<false, 3072>(sx6_1, vAo, x.srdA); SBAR();
  MMA_V(aq[11][0], cat(q8_11_0, q8_11_1), x8_0); const bf16x8 x9_1 = ld128(rd.xa[0][1] + 8192); const float d25_7 = aread1<207>(); SBAR();
  MMA_V(aq[11][1], cat(q8_11_0, q8_11_1), x8_1); const s16x4 l9_1_0 = ldtr(rd.lb[0][1] + 22528); const unsigned w25_2 = cvt_asm(d25_4, d25_5); SBAR();
  LGKM(5); MMA_V(aq[12][0], cat(q8_12_0, q8_12_1), x8_0); const s16x4 l9_1_1 = ldtr(rd.lb[0][1] + 23296); SBAR();
  MMA_V(aq[12][1], cat(q8_12_0, q8_12_1), x8_1); const s16x4 l9_2_0 = ldtr(rd.lb[0][0] + 22784); const unsigned w25_3 = cvt_asm(d25_6, d25_7); SBAR();
  MMA_V(aq[13][0], cat(q8_13_0, q8_13_1), x8_0); const s16x4 l9_2_1 = ldtr(rd.lb[0][0] + 23552); const float d26_0 = aread1<208>(); SBAR();
  MMA_V(aq[13][1], cat(q8_13_0, q8_13_1), x8_1); const s16x4 l9_3_0 = ldtr(rd.lb[0][1] + 22784); const float d26_1 = aread1<209>(); SBAR();
  MMA_V(aq[14][0], cat(q8_14_0, q8_14_1), x8_0); const s16x4 l9_3_1 = ldtr(rd.lb[0][1] + 23552); const float d26_2 = aread1<210>(); SBAR();
  MMA_V(aq[14][1], cat(q8_14_0, q8_14_1), x8_1); const s16x4 l9_4_0 = ldtr(rd.lb[0][0] + 23040); const u32x4 ww25 = {w25_0, w25_1, w25_2, w25_3}; XST(ww25, x.vS[1], x.srdS, x.s1 * 6); SBAR();
  MMA_V(aq[15][0], cat(q8_15_0, q8_15_1), x8_0); const s16x4 l9_4_1 = ldtr(rd.lb[0][0] + 23808); const float d26_3 = aread1<211>(); SBAR();
  MMA_V(aq[15][1], cat(q8_15_0, q8_15_1), x8_1); const s16x4 l9_5_0 = ldtr(rd.lb[0][1] + 23040); const unsigned w26_0 = cvt_asm(d26_0, d26_1); KEEP(ww25); SBAR();
  LGKM(5); mma_acc<0>(cat(l9_0_0, l9_0_1), x9_0); const s16x4 l9_5_1 = ldtr(rd.lb[0][1] + 23808); SBAR();
  mma_acc<4>(cat(l9_0_0, l9_0_1), x9_1); const bf16x8 gt9 = ld128(rd.gb + 576); const float d26_4 = aread1<212>(); SBAR();
  mma_acc<8>(cat(l9_1_0, l9_1_1), x9_0); asm volatile("s_waitcnt vmcnt(11)\n\ts_barrier" ::: ACLOB, "memory"); const s16x4 q9_0_0 = ldtr(rd.qb[0] + 16384); SBAR();
  mma_acc<12>(cat(l9_1_0, l9_1_1), x9_1); const s16x4 q9_0_1 = ldtr(rd.qb[0] + 18432); m0_set<28672>(x.m0Q); const unsigned sq12_0 = s_imm<3144960>(); dma_go<false, 0>(sq12_0, vQe, x.srdQ); SBAR();
  mma_acc<16>(x9_0, cat(l9_2_0, l9_2_1)); const s16x4 q9_1_0 = ldtr(rd.qb[1] + 16384); const float d26_5 = aread1<213>(); SBAR();
  mma_acc<20>(x9_1, cat(l9_2_0, l9_2_1)); const s16x4 q9_1_1 = ldtr(rd.qb[1] + 18432); const unsigned w26_1 = cvt_asm(d26_2, d26_3); SBAR();
  LGKM(5); mma_acc<24>(x9_0, cat(l9_3_0, l9_3_1)); const s16x4 q9_2_0 = ldtr(rd.qb[0] + 16640); SBAR();
  mma_acc<28>(x9_1, cat(l9_3_0, l9_3_1)); const s16x4 q9_2_1 = ldtr(rd.qb[0] + 18688); dma_go<false, 1024>(sq12_0, x.vQ, x.srdQ); SBAR();
  mma_acc<32>(x9_0, cat(l9_4_0, l9_4_1)); const s16x4 q9_3_0 = ldtr(rd.qb[1] + 16640); const unsigned sq12_1 = s_imm<3175680>(); SBAR();
  mma_acc<36>(x9_1, cat(l9_4_0, l9_4_1)); const s16x4 q9_3_1 = ldtr(rd.qb[1] + 18688); const float d26_6 = aread1<214>(); SBAR();
  mma_acc<40>(x9_0, cat(l9_5_0, l9_5_1)); const s16x4 q9_4_0 = ldtr(rd.qb[0] + 16896); const float d26_7 = aread1<215>(); SBAR();
  mma_acc<44>(x9_1, cat(l9_5_0, l9_5_1)); const s16x4 q9_4_1 = ldtr(rd.qb[0] + 18944); dma_go<false, 2048>(sq12_1, vQe, x.srdQ); SBAR();
  LGKM(5); mma_acc<48>(gt9, x9_0); const s16x4 q9_5_0 = ldtr(rd.qb[1] + 16896); SBAR();
  mma_acc<52>(gt9, x9_1); const s16x4 q9_5_1 = ldtr(rd.qb[1] + 18944); const unsigned w26_2 = cvt_asm(d26_4, d26_5); SBAR();
  MMA_V(aq[0][0], cat(q9_0_0, q9_0_1), x9_0); const s16x4 q9_6_0 = ldtr(rd.qb[0] + 17152); const unsigned w26_3 = cvt_asm(d26_6, d26_7); SBAR();
  MMA_V(aq[0][1], cat(q9_0_0, q9_0_1), x9_1); const s16x4 q9_6_1 = ldtr(rd.qb[0] + 19200); const u32x4 ww26 = {w26_0, w26_1, w26_2, w26_3}; XST(ww26, x.vS[2], x.srdS, x.s1 * 6); SBAR();
  MMA_V(aq[1][0], cat(q9_1_0, q9_1_1), x9_0); const s16x4 q9_7_0 = ldtr(rd.qb[1] + 17152); const float d27_0 = aread1<216>(); SBAR();
  MMA_V(aq[1][1], cat(q9_1_0, q9_1_1), x9_1); const s16x4 q9_7_1 = ldtr(rd.qb[1] + 19200); const float d27_1 = aread1<217>(); KEEP(ww26); SBAR();
  LGKM(5); MMA_V(aq[2][0], cat(q9_2_0, q9_2_1), x9_0); const s16x4 q9_8_0 = ldtr(rd.qb[0] + 17408); SBAR();
  MMA_V(aq[2][1], cat(q9_2_0, q9_2_1), x9_1); const s16x4 q9_8_1 = ldtr(rd.qb[0] + 19456); dma_go<false, 3072>(sq12_1, x.vQ, x.srdQ); SBAR();
  MMA_V(aq[3][0], cat(q9_3_0, q9_3_1), x9_0); const s16x4 q9_9_0 = ldtr(rd.qb[1] + 17408); const s16x4 q9_9_1 = ldtr(rd.qb[1] + 19456); SBAR();
  MMA_V(aq[3][1], cat(q9_3_0, q9_3_1), x9_1); const s16x4 q9_10_0 = ldtr(rd.qb[0] + 17664); m0_set<147456>(x.m0L); SBAR();
  MMA_V(aq[4][0], cat(q9_4_0, q9_4_1), x9_0); const s16x4 q9_10_1 = ldtr(rd.qb[0] + 19712); const unsigned sl6 = s_imm<73728>(); SBAR();
  MMA_V(aq[4][1], cat(q9_4_0, q9_4_1), x9_1); const s16x4 q9_11_0 = ldtr(rd.qb[1] + 17664); dma_go<true, 0>(sl6, x.voff, x.srdL); SBAR();
  LGKM(6); MMA_V(aq[5][0], cat(q9_5_0, q9_5_1), x9_0); const s16x4 q9_11_1 = ldtr(rd.qb[1] + 19712); const float d27_2 = aread1<218>(); SBAR();
  MMA_V(aq[5][1], cat(q9_5_0, q9_5_1), x9_1); const s16x4 q9_12_0 = ldtr(rd.qb[0] + 17920); const float d27_3 = aread1<219>(); SBAR();
  MMA_V(aq[6][0], cat(q9_6_0, q9_6_1), x9_0); const s16x4 q9_12_1 = ldtr(rd.qb[0] + 19968); const unsigned w27_0 = cvt_asm(d27_0, d27_1); SBAR();
  MMA_V(aq[6][1], cat(q9_6_0, q9_6_1), x9_1); const s16x4 q9_13_0 = ldtr(rd.qb[1] + 17920); dma_go<true, 1024>(sl6, x.voff, x.srdL); SBAR();
  MMA_V(aq[7][0], cat(q9_7_0, q9_7_1), x9_0); const s16x4 q9_13_1 = ldtr(rd.qb[1] + 19968); const float d27_4 = aread1<220>(); SBAR();
  MMA_V(aq[7][1], cat(q9_7_0, q9_7_1), x9_1); const s16x4 q9_14_0 = ldtr(rd.qb[0] + 18176); const float d27_5 = aread1<221>(); SBAR();
  LGKM(5); MMA_V(aq[8][0], cat(q9_8_0, q9_8_1), x9_0); const s16x4 q9_14_1 = ldtr(rd.qb[0] + 20224); SBAR();
  MMA_V(aq[8][1], cat(q9_8_0, q9_8_1), x9_1); const s16x4 q9_15_0 = ldtr(rd.qb[1] + 18176); dma_go<true, 2048>(sl6, x.voff, x.srdL); SBAR();
  MMA_V(aq[9][0], cat(q9_9_0, q9_9_1), x9_0); const s16x4 q9_15_1 = ldtr(rd.qb[1] + 20224); const unsigned w27_1 = cvt_asm(d27_2, d27_3); SBAR();
  MMA_V(aq[9][1], cat(q9_9_0, q9_9_1), x9_1); const bf16x8 x10_0 = ld128(rd.xa[1][0] + 0); const float d27_6 = aread1<222>(); SBAR();
  MMA_V(aq[10][0], cat(q9_10_0, q9_10_1), x9_0); const s16x4 l10_0_0 = ldtr(rd.lb[1][0] + 16384); const float d27_7 = aread1<223>(); SBAR();
  MMA_V(aq[10][1], cat(q9_10_0, q9_10_1), x9_1); const s16x4 l10_0_1 = ldtr(rd.lb[1][0] + 17152); const unsigned w27_2 = cvt_asm(d27_4, d27_5); SBAR();
  MMA_V(aq[11][0], cat(q9_11_0, q9_11_1), x9_0); const bf16x8 x10_1 = ld128(rd.xa[1][0] + 8192); const unsigned w27_3 = cvt_asm(d27_6, d27_7); SBAR();
  MMA_V(aq[11][1], cat(q9_11_0, q9_11_1), x9_1); const s16x4 l10_1_0 = ldtr(rd.lb[1][1] + 16384); const float d28_0 = aread1<224>(); SBAR();
  LGKM(5); MMA_V(aq[12][0], cat(q9_12_0, q9_12_1), x9_0); const s16x4 l10_1_1 = ldtr(rd.lb[1][1] + 17152); SBAR();
  MMA_V(aq[12][1], cat(q9_12_0, q9_12_1), x9_1); const s16x4 l10_2_0 = ldtr(rd.lb[1][0] + 16640); const u32x4 ww27 = {w27_0, w27_1, w27_2, w27_3}; XST(ww27, x.vS[3], x.srdS, x.s1 * 6); SBAR();
  MMA_V(aq[13][0], cat(q9_13_0, q9_13_1), x9_0); const s16x4 l10_2_1 = ldtr(rd.lb[1][0] + 17408); const float d28_1 = aread1<225>(); SBAR();
  MMA_V(aq[13][1], cat(q9_13_0, q9_13_1), x9_1); const s16x4 l10_3_0 = ldtr(rd.lb[1][1] + 16640); const float d28_2 = aread1<226>(); KEEP(ww27); SBAR();
  MMA_V(aq[14][0], cat(q9_14_0, q9_14_1), x9_0); const s16x4 l10_3_1 = ldtr(rd.lb[1][1] + 17408); const float d28_3 = aread1<227>(); SBAR();
  MMA_V(aq[14][1], cat(q9_14_0, q9_14_1), x9_1); const s16x4 l10_4_0 = ldtr(rd.lb[1][0] + 16896); const unsigned w28_0 = cvt_asm(d28_0, d28_1); SBAR();
  MMA_V(aq[15][0], cat(q9_15_0, q9_15_1), x9_0); const s16x4 l10_4_1 = ldtr(rd.lb[1][0] + 17664); const float d28_4 = aread1<228>(); SBAR();
  MMA_V(aq[15][1], cat(q9_15_0, q9_15_1), x9_1); const s16x4 l10_5_0 = ldtr(rd.lb[1][1] + 16896); const float d28_5 = aread1<229>(); SBAR();
  LGKM(5); mma_acc<0>(cat(l10_0_0, l10_0_1), x10_0); const s16x4 l10_5_1 = ldtr(rd.lb[1][1] + 17664); SBAR();
  mma_acc<4>(cat(l10_0_0, l10_0_1), x10_1); const bf16x8 gt10 = ld128(rd.gb + 640); const unsigned w28_1 = cvt_asm(d28_2, d28_3); SBAR();
  mma_acc<8>(cat(l10_1_0, l10_1_1), x10_0); asm volatile("s_waitcnt vmcnt(20)\n\ts_barrier" ::: ACLOB, "memory"); const s16x4 q10_0_0 = ldtr(rd.qb[0] + 36864); SBAR();
  mma_acc<12>(cat(l10_1_0, l10_1_1), x10_1); const s16x4 q10_0_1 = ldtr(rd.qb[0] + 38912); m0_set<0>(x.m0Q); const unsigned sq13_0 = s_imm<3407104>(); dma_go<false, 0>(sq13_0, vQe, x.srdQ); SBAR();
  mma_acc<16>(x10_0, cat(l10_2_0, l10_2_1)); const s16x4 q10_1_0 = ldtr(rd.qb[1] + 36864); const float d28_6 = aread1<230>(); SBAR();
  mma_acc<20>(x10_1, cat(l10_2_0, l10_2_1)); const s16x4 q10_1_1 = ldtr(rd.qb[1] + 38912); const float d28_7 = aread1<231>(); SBAR();
  LGKM(5); mma_acc<24>(x10_0, cat(l10_3_0, l10_3_1)); const s16x4 q10_2_0 = ldtr(rd.qb[0] + 37120); SBAR();
  mma_acc<28>(x10_1, cat(l10_3_0, l10_3_1)); const s16x4 q10_2_1 = ldtr(rd.qb[0] + 39168); dma_go<false, 1024>(sq13_0, x.vQ, x.srdQ); SBAR();
  mma_acc<32>(x10_0, cat(l10_4_0, l10_4_1)); const s16x4 q10_3_0 = ldtr(rd.qb[1] + 37120); const unsigned sq13_1 = s_imm<3437824>(); SBAR();
  mma_acc<36>(x10_1, cat(l10_4_0, l10_4_1)); const s16x4 q10_3_1 = ldtr(rd.qb[1] + 39168); const unsigned w28_2 = cvt_asm(d28_4, d28_5); SBAR();
  mma_acc<40>(x10_0, cat(l10_5_0, l10_5_1)); const s16x4 q10_4_0 = ldtr(rd.qb[0] + 37376); const unsigned w28_3 = cvt_asm(d28_6, d28_7); SBAR();
  mma_acc<44>(x10_1, cat(l10_5_0, l10_5_1)); const s16x4 q10_4_1 = ldtr(rd.qb[0] + 39424); const u32x4 ww28 = {w28_0, w28_1, w28_2, w28_3}; XST(ww28, x.vS[0], x.srdS, x.s1 * 7); SBAR();
  LGKM(5); mma_acc<48>(gt10, x10_0); const s16x4 q10_5_0 = ldtr(rd.qb[1] + 37376); SBAR();
  mma_acc<52>(gt10, x10_1); const s16x4 q10_5_1 = ldtr(rd.qb[1] + 39424); const float d29_0 = aread1<232>(); KEEP(ww28); SBAR();
  MMA_V(aq[0][0], cat(q10_0_0, q10_0_1), x10_0); const s16x4 q10_6_0 = ldtr(rd.qb[0] + 37632); const float d29_1 = aread1<233>(); SBAR();
  MMA_V(aq[0][1], cat(q10_0_0, q10_0_1), x10_1); const s16x4 q10_6_1 = ldtr(rd.qb[0] + 39680); dma_go<false, 2048>(sq13_1, vQe, x.srdQ); SBAR();
  MMA_V(aq[1][0], cat(q10_1_0, q10_1_1), x10_0); const s16x4 q10_7_0 = ldtr(rd.qb[1] + 37632); const float d29_2 = aread1<234>(); SBAR();
  MMA_V(aq[1][1], cat(q10_1_0, q10_1_1), x10_1); const s16x4 q10_7_1 = ldtr(rd.qb[1] + 39680); const float d29_3 = aread1<235>(); SBAR();
  LGKM(5); MMA_V(aq[2][0], cat(q10_2_0, q10_2_1), x10_0); const s16x4 q10_8_0 = ldtr(rd.qb[0] + 37888); SBAR();
  MMA_V(aq[2][1], cat(q10_2_0, q10_2_1), x10_1); const s16x4 q10_8_1 = ldtr(rd.qb[0] + 39936); dma_go<false, 3072>(sq13_1, x.vQ, x.srdQ); SBAR();
  MMA_V(aq[3][0], cat(q10_3_0, q10_3_1), x10_0); const s16x4 q10_9_0 = ldtr(rd.qb[1] + 37888); const s16x4 q10_9_1 = ldtr(rd.qb[1] + 39936); SBAR();
  MMA_V(aq[3][1], cat(q10_3_0, q10_3_1), x10_1); const s16x4 q10_10_0 = ldtr(rd.qb[0] + 38144); const unsigned w29_0 = cvt_asm(d29_0, d29_1); SBAR();
  MMA_V(aq[4][0], cat(q10_4_0, q10_4_1), x10_0); const s16x4 q10_10_1 = ldtr(rd.qb[0] + 40192); const float d29_4 = aread1<236>(); SBAR();
  MMA_V(aq[4][1], cat(q10_4_0, q10_4_1), x10_1); const s16x4 q10_11_0 = ldtr(rd.qb[1] + 38144); const float d29_5 = aread1<237>(); SBAR();
  LGKM(6); MMA_V(aq[5][0], cat(q10_5_0, q10_5_1), x10_0); const s16x4 q10_11_1 = ldtr(rd.qb[1] + 40192); SBAR();
  MMA_V(aq[5][1], cat(q10_5_0, q10_5_1), x10_1); const s16x4 q10_12_0 = ldtr(rd.qb[0] + 38400); const unsigned w29_1 = cvt_asm(d29_2, d29_3); SBAR();
  MMA_V(aq[6][0], cat(q10_6_0, q10_6_1), x10_0); const s16x4 q10_12_1 = ldtr(rd.qb[0] + 40448); const float d29_6 = aread1<238>(); SBAR();
  MMA_V(aq[6][1], cat(q10_6_0, q10_6_1), x10_1); const s16x4 q10_13_0 = ldtr(rd.qb[1] + 38400); const float d29_7 = aread1<239>(); SBAR();
  MMA_V(aq[7][0], cat(q10_7_0, q10_7_1), x10_0); const s16x4 q10_13_1 = ldtr(rd.qb[1] + 40448); const unsigned w29_2 = cvt_asm(d29_4, d29_5); SBAR();
  MMA_V(aq[7][1], cat(q10_7_0, q10_7_1), x10_1); const s16x4 q10_14_0 = ldtr(rd.qb[0] + 38656); const unsigned w29_3 = cvt_asm(d29_6, d29_7); SBAR();
  LGKM(5); MMA_V(aq[8][0], cat(q10_8_0, q10_8_1), x10_0); const s16x4 q10_14_1 = ldtr(rd.qb[0] + 40704); SBAR();
  MMA_V(aq[8][1], cat(q10_8_0, q10_8_1), x10_1); const s16x4 q10_15_0 = ldtr(rd.qb[1] + 38656); const u32x4 ww29 = {w29_0, w29_1, w29_2, w29_3}; XST(ww29, x.vS[1], x.srdS, x.s1 * 7); SBAR();
  MMA_V(aq[9][0], cat(q10_9_0, q10_9_1), x10_0); const s16x4 q10_15_1 = ldtr(rd.qb[1] + 40704); const float d30_0 = aread1<240>(); SBAR();
  MMA_V(aq[9][1], cat(q10_9_0, q10_9_1), x10_1); const bf16x8 x11_0 = ld128(rd.xa[1][1] + 0); const float d30_1 = aread1<241>(); KEEP(ww29); SBAR();
  MMA_V(aq[10][0], cat(q10_10_0, q10_10_1), x10_0); const s16x4 l11_0_0 = ldtr(rd.lb[1][0] + 22528); const float d30_2 = aread1<242>(); SBAR();
  MMA_V(aq[10][1], cat(q10_10_0, q10_10_1), x10_1); const s16x4 l11_0_1 = ldtr(rd.lb[1][0] + 23296); const float d30_3 = aread1<243>(); SBAR();
  MMA_V(aq[11][0], cat(q10_11_0, q10_11_1), x10_0); const bf16x8 x11_1 = ld128(rd.xa[1][1] + 8192); const unsigned w30_0 = cvt_asm(d30_0, d30_1); SBAR();
  MMA_V(aq[11][1], cat(q10_11_0, q10_11_1), x10_1); const s16x4 l11_1_0 = ldtr(rd.lb[1][1] + 22528); const float d30_4 = aread1<244>(); SBAR();
  LGKM(5); MMA_V(aq[12][0], cat(q10_12_0, q10_12_1), x10_0); const s16x4 l11_1_1 = ldtr(rd.lb[1][1] + 23296); SBAR();
  MMA_V(aq[12][1], cat(q10_12_0, q10_12_1), x10_1); const s16x4 l11_2_0 = ldtr(rd.lb[1][0] + 22784); const float d30_5 = aread1<245>(); SBAR();
  MMA_V(aq[13][0], cat(q10_13_0, q10_13_1), x10_0); const s16x4 l11_2_1 = ldtr(rd.lb[1][0] + 23552); const unsigned w30_1 = cvt_asm(d30_2, d30_3); SBAR();
  MMA_V(aq[13][1], cat(q10_13_0, q10_13_1), x10_1); const s16x4 l11_3_0 = ldtr(rd.lb[1][1] + 22784); const float d30_6 = aread1<246>(); SBAR();
  MMA_V(aq[14][0], cat(q10_14_0, q10_14_1), x10_0); const s16x4 l11_3_1 = ldtr(rd.lb[1][1] + 23552); const float d30_7 = aread1<247>(); SBAR();
  MMA_V(aq[14][1], cat(q10_14_0, q10_14_1), x10_1); const s16x4 l11_4_0 = ldtr(rd.lb[1][0] + 23040); const unsigned w30_2 = cvt_asm(d30_4, d30_5); SBAR();
  MMA_V(aq[15][0], cat(q10_15_0, q10_15_1), x10_0); const s16x4 l11_4_1 = ldtr(rd.lb[1][0] + 23808); const unsigned w30_3 = cvt_asm(d30_6, d30_7); SBAR();
  MMA_V(aq[15][1], cat(q10_15_0, q10_15_1), x10_1); const s16x4 l11_5_0 = ldtr(rd.lb[1][1] + 23040); const float d31_0 = aread1<248>(); SBAR();
  LGKM(5); mma_acc<0>(cat(l11_0_0, l11_0_1), x11_0); const s16x4 l11_5_1 = ldtr(rd.lb[1][1] + 23808); SBAR();
  mma_acc<4>(cat(l11_0_0, l11_0_1), x11_1); const bf16x8 gt11 = ld128(rd.gb + 704); const float d31_1 = aread1<249>(); SBAR();
  mma_acc<8>(cat(l11_1_0, l11_1_1), x11_0); asm volatile("s_waitcnt vmcnt(7)\n\ts_barrier" ::: ACLOB, "memory"); const s16x4 q11_0_0 = ldtr(rd.qb[0] + 53248); SBAR();
  mma_acc<12>(cat(l11_1_0, l11_1_1), x11_1); const s16x4 q11_0_1 = ldtr(rd.qb[0] + 55296); const u32x4 ww30 = {w30_0, w30_1, w30_2, w30_3}; XST(ww30, x.vS[2], x.srdS, x.s1 * 7); SBAR();
  mma_acc<16>(x11_0, cat(l11_2_0, l11_2_1)); const s16x4 q11_1_0 = ldtr(rd.qb[1] + 53248); const float d31_2 = aread1<250>(); SBAR();
  mma_acc<20>(x11_1, cat(l11_2_0, l11_2_1)); const s16x4 q11_1_1 = ldtr(rd.qb[1] + 55296); const float d31_3 = aread1<251>(); KEEP(ww30); SBAR();
  LGKM(5); mma_acc<24>(x11_0, cat(l11_3_0, l11_3_1)); const s16x4 q11_2_0 = ldtr(rd.qb[0] + 53504); SBAR();
  mma_acc<28>(x11_1, cat(l11_3_0, l11_3_1)); const s16x4 q11_2_1 = ldtr(rd.qb[0] + 55552); const unsigned w31_0 = cvt_asm(d31_0, d31_1); SBAR();
  mma_acc<32>(x11_0, cat(l11_4_0, l11_4_1)); const s16x4 q11_3_0 = ldtr(rd.qb[1] + 53504); const float d31_4 = aread1<252>(); SBAR();
  mma_acc<36>(x11_1, cat(l11_4_0, l11_4_1)); const s16x4 q11_3_1 = ldtr(rd.qb[1] + 55552); const float d31_5 = aread1<253>(); SBAR();
  mma_acc<40>(x11_0, cat(l11_5_0, l11_5_1)); const s16x4 q11_4_0 = ldtr(rd.qb[0] + 53760); const unsigned w31_1 = cvt_asm(d31_2, d31_3); SBAR();
  mma_acc<44>(x11_1, cat(l11_5_0, l11_5_1)); const s16x4 q11_4_1 = ldtr(rd.qb[0] + 55808); const float d31_6 = aread1<254>(); SBAR();
  LGKM(5); mma_acc<48>(gt11, x11_0); const s16x4 q11_5_0 = ldtr(rd.qb[1] + 53760); SBAR();
  mma_acc<52>(gt11, x11_1); const s16x4 q11_5_1 = ldtr(rd.qb[1] + 55808); const float d31_7 = aread1<255>(); SBAR();
  MMA_V(aq[0][0], cat(q11_0_0, q11_0_1), x11_0); const s16x4 q11_6_0 = ldtr(rd.qb[0] + 54016); const unsigned w31_2 = cvt_asm(d31_4, d31_5); SBAR();
  MMA_V(aq[0][1], cat(q11_0_0, q11_0_1), x11_1); const s16x4 q11_6_1 = ldtr(rd.qb[0] + 56064); const unsigned w31_3 = cvt_asm(d31_6, d31_7); SBAR();
  MMA_V(aq[1][0], cat(q11_1_0, q11_1_1), x11_0); const s16x4 q11_7_0 = ldtr(rd.qb[1] + 54016); SBAR();
  MMA_V(aq[1][1], cat(q11_1_0, q11_1_1), x11_1); const s16x4 q11_7_1 = ldtr(rd.qb[1] + 56064); SBAR();
  LGKM(5); MMA_V(aq[2][0], cat(q11_2_0, q11_2_1), x11_0); const s16x4 q11_8_0 = ldtr(rd.qb[0] + 54272); SBAR();
  MMA_V(aq[2][1], cat(q11_2_0, q11_2_1), x11_1); const s16x4 q11_8_1 = ldtr(rd.qb[0] + 56320); const u32x4 ww31 = {w31_0, w31_1, w31_2, w31_3}; XST(ww31, x.vS[3], x.srdS, x.s1 * 7); SBAR();
  MMA_V(aq[3][0], cat(q11_3_0, q11_3_1), x11_0); const s16x4 q11_9_0 = ldtr(rd.qb[1] + 54272); const s16x4 q11_9_1 = ldtr(rd.qb[1] + 56320); SBAR();
  MMA_V(aq[3][1], cat(q11_3_0, q11_3_1), x11_1); const s16x4 q11_10_0 = ldtr(rd.qb[0] + 54528); KEEP(ww31); SBAR();
  MMA_V(aq[4][0], cat(q11_4_0, q11_4_1), x11_0); const s16x4 q11_10_1 = ldtr(rd.qb[0] + 56576); SBAR();
  MMA_V(aq[4][1], cat(q11_4_0, q11_4_1), x11_1); const s16x4 q11_11_0 = ldtr(rd.qb[1] + 54528); SBAR();
  LGKM(6); MMA_V(aq[5][0], cat(q11_5_0, q11_5_1), x11_0); const s16x4 q11_11_1 = ldtr(rd.qb[1] + 56576); SBAR();
  MMA_V(aq[5][1], cat(q11_5_0, q11_5_1), x11_1); const s16x4 q11_12_0 = ldtr(rd.qb[0] + 54784); SBAR();
  MMA_V(aq[6][0], cat(q11_6_0, q11_6_1), x11_0); const s16x4 q11_12_1 = ldtr(rd.qb[0] + 56832); SBAR();
  MMA_V(aq[6][1], cat(q11_6_0, q11_6_1), x11_1); const s16x4 q11_13_0 = ldtr(rd.qb[1] + 54784); SBAR();
  MMA_V(aq[7][0], cat(q11_7_0, q11_7_1), x11_0); const s16x4 q11_13_1 = ldtr(rd.qb[1] + 56832); SBAR();
  MMA_V(aq[7][1], cat(q11_7_0, q11_7_1), x11_1); const s16x4 q11_14_0 = ldtr(rd.qb[0] + 55040); SBAR();
  LGKM(5); MMA_V(aq[8][0], cat(q11_8_0, q11_8_1), x11_0); const s16x4 q11_14_1 = ldtr(rd.qb[0] + 57088); SBAR();
  MMA_V(aq[8][1], cat(q11_8_0, q11_8_1), x11_1); const s16x4 q11_15_0 = ldtr(rd.qb[1] + 55040); SBAR();
  MMA_V(aq[9][0], cat(q11_9_0, q11_9_1), x11_0); const s16x4 q11_15_1 = ldtr(rd.qb[1] + 57088); SBAR();
  MMA_V(aq[9][1], cat(q11_9_0, q11_9_1), x11_1); const bf16x8 x12_0 = ld128(rd.xa[1][0] + 32768); SBAR();
  MMA_V(aq[10][0], cat(q11_10_0, q11_10_1), x11_0); const s16x4 l12_0_0 = ldtr(rd.lb[1][0] + 49152); SBAR();
  MMA_V(aq[10][1], cat(q11_10_0, q11_10_1), x11_1); const s16x4 l12_0_1 = ldtr(rd.lb[1][0] + 49920); SBAR();
  MMA_V(aq[11][0], cat(q11_11_0, q11_11_1), x11_0); const bf16x8 x12_1 = ld128(rd.xa[1][0] + 40960); SBAR();
  MMA_V(aq[11][1], cat(q11_11_0, q11_11_1), x11_1); const s16x4 l12_1_0 = ldtr(rd.lb[1][1] + 49152); SBAR();
  LGKM(5); MMA_V(aq[12][0], cat(q11_12_0, q11_12_1), x11_0); const s16x4 l12_1_1 = ldtr(rd.lb[1][1] + 49920); SBAR();
  MMA_V(aq[12][1], cat(q11_12_0, q11_12_1), x11_1); const s16x4 l12_2_0 = ldtr(rd.lb[1][0] + 49408); SBAR();
  MMA_V(aq[13][0], cat(q11_13_0, q11_13_1), x11_0); const s16x4 l12_2_1 = ldtr(rd.lb[1][0] + 50176); SBAR();
  MMA_V(aq[13][1], cat(q11_13_0, q11_13_1), x11_1); const s16x4 l12_3_0 = ldtr(rd.lb[1][1] + 49408); SBAR();
  MMA_V(aq[14][0], cat(q11_14_0, q11_14_1), x11_0); const s16x4 l12_3_1 = ldtr(rd.lb[1][1] + 50176); SBAR();
  MMA_V(aq[14][1], cat(q11_14_0, q11_14_1), x11_1); const s16x4 l12_4_0 = ldtr(rd.lb[1][0] + 49664); SBAR();
  MMA_V(aq[15][0], cat(q11_15_0, q11_15_1), x11_0); const s16x4 l12_4_1 = ldtr(rd.lb[1][0] + 50432); SBAR();
  MMA_V(aq[15][1], cat(q11_15_0, q11_15_1), x11_1); const s16x4 l12_5_0 = ldtr(rd.lb[1][1] + 49664); SBAR();
  LGKM(5); mma_acc<0>(cat(l12_0_0, l12_0_1), x12_0); const s16x4 l12_5_1 = ldtr(rd.lb[1][1] + 50432); SBAR();
  mma_acc<4>(cat(l12_0_0, l12_0_1), x12_1); const bf16x8 gt12 = ld128(rd.gb + 768); SBAR();
  mma_acc<8>(cat(l12_1_0, l12_1_1), x12_0); asm volatile("s_waitcnt vmcnt(9)\n\ts_barrier" ::: ACLOB, "memory");
  X_QLAST(); const s16x4 q12_0_0 = ldtr(rd.qb[0] + 0); SBAR();
  mma_acc<12>(cat(l12_1_0, l12_1_1), x12_1); const s16x4 q12_0_1 = ldtr(rd.qb[0] + 2048); SBAR();
  mma_acc<16>(x12_0, cat(l12_2_0, l12_2_1)); const s16x4 q12_1_0 = ldtr(rd.qb[1] + 0); SBAR();
  mma_acc<20>(x12_1, cat(l12_2_0, l12_2_1)); const s16x4 q12_1_1 = ldtr(rd.qb[1] + 2048); SBAR();
  LGKM(5); mma_acc<24>(x12_0, cat(l12_3_0, l12_3_1)); const s16x4 q12_2_0 = ldtr(rd.qb[0] + 256); SBAR();
  mma_acc<28>(x12_1, cat(l12_3_0, l12_3_1)); const s16x4 q12_2_1 = ldtr(rd.qb[0] + 2304); SBAR();
  mma_acc<32>(x12_0, cat(l12_4_0, l12_4_1)); const s16x4 q12_3_0 = ldtr(rd.qb[1] + 256); SBAR();
  mma_acc<36>(x12_1, cat(l12_4_0, l12_4_1)); const s16x4 q12_3_1 = ldtr(rd.qb[1] + 2304); SBAR();
  mma_acc<40>(x12_0, cat(l12_5_0, l12_5_1)); const s16x4 q12_4_0 = ldtr(rd.qb[0] + 512); SBAR();
  mma_acc<44>(x12_1, cat(l12_5_0, l12_5_1)); const s16x4 q12_4_1 = ldtr(rd.qb[0] + 2560); SBAR();
  LGKM(5); mma_acc<48>(gt12, x12_0); const s16x4 q12_5_0 = ldtr(rd.qb[1] + 512); SBAR();
  mma_acc<52>(gt12, x12_1); const s16x4 q12_5_1 = ldtr(rd.qb[1] + 2560); SBAR();
  MMA_V(aq[0][0], cat(q12_0_0, q12_0_1), x12_0); const s16x4 q12_6_0 = ldtr(rd.qb[0] + 768); SBAR();
  MMA_V(aq[0][1], cat(q12_0_0, q12_0_1), x12_1); const s16x4 q12_6_1 = ldtr(rd.qb[0] + 2816); SBAR();
  MMA_V(aq[1][0], cat(q12_1_0, q12_1_1), x12_0); const s16x4 q12_7_0 = ldtr(rd.qb[1] + 768); SBAR();
  MMA_V(aq[1][1], cat(q12_1_0, q12_1_1), x12_1); const s16x4 q12_7_1 = ldtr(rd.qb[1] + 2816); SBAR();
  LGKM(5); MMA_V(aq[2][0], cat(q12_2_0, q12_2_1), x12_0); const s16x4 q12_8_0 = ldtr(rd.qb[0] + 1024); SBAR();
  MMA_V(aq[2][1], cat(q12_2_0, q12_2_1), x12_1); const s16x4 q12_8_1 = ldtr(rd.qb[0] + 3072); SBAR();
  MMA_V(aq[3][0], cat(q12_3_0, q12_3_1), x12_0); const s16x4 q12_9_0 = ldtr(rd.qb[1] + 1024); const s16x4 q12_9_1 = ldtr(rd.qb[1] + 3072); SBAR();
  MMA_V(aq[3][1], cat(q12_3_0, q12_3_1), x12_1); const s16x4 q12_10_0 = ldtr(rd.qb[0] + 1280); SBAR();
  MMA_V(aq[4][0], cat(q12_4_0, q12_4_1), x12_0); const s16x4 q12_10_1 = ldtr(rd.qb[0] + 3328); SBAR();
  MMA_V(aq[4][1], cat(q12_4_0, q12_4_1), x12_1); const s16x4 q12_11_0 = ldtr(rd.qb[1] + 1280); SBAR();
  LGKM(6); MMA_V(aq[5][0], cat(q12_5_0, q12_5_1), x12_0); const s16x4 q12_11_1 = ldtr(rd.qb[1] + 3328); SBAR();
  MMA_V(aq[5][1], cat(q12_5_0, q12_5_1), x12_1); const s16x4 q12_12_0 = ldtr(rd.qb[0] + 1536); SBAR();
  MMA_V(aq[6][0], cat(q12_6_0, q12_6_1), x12_0); const s16x4 q12_12_1 = ldtr(rd.qb[0] + 3584); SBAR();
  MMA_V(aq[6][1], cat(q12_6_0, q12_6_1), x12_1); const s16x4 q12_13_0 = ldtr(rd.qb[1] + 1536); SBAR();
  MMA_V(aq[7][0], cat(q12_7_0, q12_7_1), x12_0); const s16x4 q12_13_1 = ldtr(rd.qb[1] + 3584); SBAR();
  MMA_V(aq[7][1], cat(q12_7_0, q12_7_1), x12_1); const s16x4 q12_14_0 = ldtr(rd.qb[0] + 1792); SBAR();
  LGKM(5); MMA_V(aq[8][0], cat(q12_8_0, q12_8_1), x12_0); const s16x4 q12_14_1 = ldtr(rd.qb[0] + 3840); SBAR();
  MMA_V(aq[8][1], cat(q12_8_0, q12_8_1), x12_1); const s16x4 q12_15_0 = ldtr(rd.qb[1] + 1792); SBAR();
  MMA_V(aq[9][0], cat(q12_9_0, q12_9_1), x12_0); const s16x4 q12_15_1 = ldtr(rd.qb[1] + 3840); SBAR();
  MMA_V(aq[9][1], cat(q12_9_0, q12_9_1), x12_1); const bf16x8 x13_0 = ld128(rd.xa[1][1] + 32768); SBAR();
  MMA_V(aq[10][0], cat(q12_10_0, q12_10_1), x12_0); const s16x4 l13_0_0 = ldtr(rd.lb[1][0] + 55296); SBAR();
  MMA_V(aq[10][1], cat(q12_10_0, q12_10_1), x12_1); const s16x4 l13_0_1 = ldtr(rd.lb[1][0] + 56064); SBAR();
  MMA_V(aq[11][0], cat(q12_11_0, q12_11_1), x12_0); const bf16x8 x13_1 = ld128(rd.xa[1][1] + 40960); SBAR();
  MMA_V(aq[11][1], cat(q12_11_0, q12_11_1), x12_1); const s16x4 l13_1_0 = ldtr(rd.lb[1][1] + 55296); SBAR();
  LGKM(5); MMA_V(aq[12][0], cat(q12_12_0, q12_12_1), x12_0); const s16x4 l13_1_1 = ldtr(rd.lb[1][1] + 56064); SBAR();
  MMA_V(aq[12][1], cat(q12_12_0, q12_12_1), x12_1); const s16x4 l13_2_0 = ldtr(rd.lb[1][0] + 55552); SBAR();
  MMA_V(aq[13][0], cat(q12_13_0, q12_13_1), x12_0); const s16x4 l13_2_1 = ldtr(rd.lb[1][0] + 56320); SBAR();
  MMA_V(aq[13][1], cat(q12_13_0, q12_13_1), x12_1); const s16x4 l13_3_0 = ldtr(rd.lb[1][1] + 55552); SBAR();
  MMA_V(aq[14][0], cat(q12_14_0, q12_14_1), x12_0); const s16x4 l13_3_1 = ldtr(rd.lb[1][1] + 56320); SBAR();
  MMA_V(aq[14][1], cat(q12_14_0, q12_14_1), x12_1); const s16x4 l13_4_0 = ldtr(rd.lb[1][0] + 55808); SBAR();
  MMA_V(aq[15][0], cat(q12_15_0, q12_15_1), x12_0); const s16x4 l13_4_1 = ldtr(rd.lb[1][0] + 56576); SBAR();
  MMA_V(aq[15][1], cat(q12_15_0, q12_15_1), x12_1); const s16x4 l13_5_0 = ldtr(rd.lb[1][1] + 55808); SBAR();
  LGKM(5); mma_acc<0>(cat(l13_0_0, l13_0_1), x13_0); const s16x4 l13_5_1 = ldtr(rd.lb[1][1] + 56576); SBAR();
  mma_acc<4>(cat(l13_0_0, l13_0_1), x13_1); const bf16x8 gt13 = ld128(rd.gb + 832); SBAR();
  mma_acc<8>(cat(l13_1_0, l13_1_1), x13_0); asm volatile("s_waitcnt vmcnt(0)\n\ts_barrier" ::: ACLOB, "memory"); X_PUBLISH_KV(); const s16x4 q13_0_0 = ldtr(qlast[0] + 0); SBAR();
  mma_acc<12>(cat(l13_1_0, l13_1_1), x13_1); const s16x4 q13_0_1 = ldtr(qlast[0] + 2048); SBAR();
  mma_acc<16>(x13_0, cat(l13_2_0, l13_2_1)); const s16x4 q13_1_0 = ldtr(qlast[1] + 0); SBAR();
  mma_acc<20>(x13_1, cat(l13_2_0, l13_2_1)); const s16x4 q13_1_1 = ldtr(qlast[1] + 2048); SBAR();
  LGKM(5); mma_acc<24>(x13_0, cat(l13_3_0, l13_3_1)); const s16x4 q13_2_0 = ldtr(qlast[0] + 256); SBAR();
  mma_acc<28>(x13_1, cat(l13_3_0, l13_3_1)); const s16x4 q13_2_1 = ldtr(qlast[0] + 2304); SBAR();
  mma_acc<32>(x13_0, cat(l13_4_0, l13_4_1)); const s16x4 q13_3_0 = ldtr(qlast[1] + 256); SBAR();
  mma_acc<36>(x13_1, cat(l13_4_0, l13_4_1)); const s16x4 q13_3_1 = ldtr(qlast[1] + 2304); SBAR();
  mma_acc<40>(x13_0, cat(l13_5_0, l13_5_1)); const s16x4 q13_4_0 = ldtr(qlast[0] + 512); SBAR();
  mma_acc<44>(x13_1, cat(l13_5_0, l13_5_1)); const s16x4 q13_4_1 = ldtr(qlast[0] + 2560); SBAR();
  LGKM(5); mma_acc<48>(gt13, x13_0); const s16x4 q13_5_0 = ldtr(qlast[1] + 512); SBAR();
  mma_acc<52>(gt13, x13_1); const s16x4 q13_5_1 = ldtr(qlast[1] + 2560); SBAR();
  MMA_V(aq[0][0], cat(q13_0_0, q13_0_1), x13_0); const s16x4 q13_6_0 = ldtr(qlast[0] + 768); SBAR();
  MMA_V(aq[0][1], cat(q13_0_0, q13_0_1), x13_1); const s16x4 q13_6_1 = ldtr(qlast[0] + 2816); SBAR();
  MMA_V(aq[1][0], cat(q13_1_0, q13_1_1), x13_0); const s16x4 q13_7_0 = ldtr(qlast[1] + 768); SBAR();
  MMA_V(aq[1][1], cat(q13_1_0, q13_1_1), x13_1); const s16x4 q13_7_1 = ldtr(qlast[1] + 2816); SBAR();
  LGKM(5); MMA_V(aq[2][0], cat(q13_2_0, q13_2_1), x13_0); const s16x4 q13_8_0 = ldtr(qlast[0] + 1024); SBAR();
  MMA_V(aq[2][1], cat(q13_2_0, q13_2_1), x13_1); const s16x4 q13_8_1 = ldtr(qlast[0] + 3072); SBAR();
  MMA_V(aq[3][0], cat(q13_3_0, q13_3_1), x13_0); const s16x4 q13_9_0 = ldtr(qlast[1] + 1024); const s16x4 q13_9_1 = ldtr(qlast[1] + 3072); SBAR();
  MMA_V(aq[3][1], cat(q13_3_0, q13_3_1), x13_1); const s16x4 q13_10_0 = ldtr(qlast[0] + 1280); SBAR();
  MMA_V(aq[4][0], cat(q13_4_0, q13_4_1), x13_0); const s16x4 q13_10_1 = ldtr(qlast[0] + 3328); SBAR();
  MMA_V(aq[4][1], cat(q13_4_0, q13_4_1), x13_1); const s16x4 q13_11_0 = ldtr(qlast[1] + 1280); SBAR();
  LGKM(6); MMA_V(aq[5][0], cat(q13_5_0, q13_5_1), x13_0); const s16x4 q13_11_1 = ldtr(qlast[1] + 3328); SBAR();
  MMA_V(aq[5][1], cat(q13_5_0, q13_5_1), x13_1); const s16x4 q13_12_0 = ldtr(qlast[0] + 1536); SBAR();
  MMA_V(aq[6][0], cat(q13_6_0, q13_6_1), x13_0); const s16x4 q13_12_1 = ldtr(qlast[0] + 3584); SBAR();
  MMA_V(aq[6][1], cat(q13_6_0, q13_6_1), x13_1); const s16x4 q13_13_0 = ldtr(qlast[1] + 1536); SBAR();
  MMA_V(aq[7][0], cat(q13_7_0, q13_7_1), x13_0); const s16x4 q13_13_1 = ldtr(qlast[1] + 3584); SBAR();
  MMA_V(aq[7][1], cat(q13_7_0, q13_7_1), x13_1); const s16x4 q13_14_0 = ldtr(qlast[0] + 1792); SBAR();
  LGKM(5); MMA_V(aq[8][0], cat(q13_8_0, q13_8_1), x13_0); const s16x4 q13_14_1 = ldtr(qlast[0] + 3840); SBAR();
  MMA_V(aq[8][1], cat(q13_8_0, q13_8_1), x13_1); const s16x4 q13_15_0 = ldtr(qlast[1] + 1792); SBAR();
  MMA_V(aq[9][0], cat(q13_9_0, q13_9_1), x13_0); const s16x4 q13_15_1 = ldtr(qlast[1] + 3840); SBAR();
  MMA_V(aq[9][1], cat(q13_9_0, q13_9_1), x13_1); SBAR();
  MMA_V(aq[10][0], cat(q13_10_0, q13_10_1), x13_0); SBAR();
  MMA_V(aq[10][1], cat(q13_10_0, q13_10_1), x13_1); SBAR();
  MMA_V(aq[11][0], cat(q13_11_0, q13_11_1), x13_0); SBAR();
  MMA_V(aq[11][1], cat(q13_11_0, q13_11_1), x13_1); SBAR();
  LGKM(0); MMA_V(aq[12][0], cat(q13_12_0, q13_12_1), x13_0); SBAR();
  MMA_V(aq[12][1], cat(q13_12_0, q13_12_1), x13_1); SBAR();
  MMA_V(aq[13][0], cat(q13_13_0, q13_13_1), x13_0); SBAR();
  MMA_V(aq[13][1], cat(q13_13_0, q13_13_1), x13_1); SBAR();
  MMA_V(aq[14][0], cat(q13_14_0, q13_14_1), x13_0); SBAR();
  MMA_V(aq[14][1], cat(q13_14_0, q13_14_1), x13_1); SBAR();
  MMA_V(aq[15][0], cat(q13_15_0, q13_15_1), x13_0); SBAR();
  MMA_V(aq[15][1], cat(q13_15_0, q13_15_1), x13_1); SBAR();

  asm volatile("s_nop 7\n\ts_nop 1" ::: ACLOB, "memory");
#pragma unroll
  for (int n = 0; n < 16; ++n) { PIN_V(aq[n][0]); PIN_V(aq[n][1]); }
  SBAR();
  const rsrc_t srdQo = mk_rsrc(w.ws + WS_Q + ((size_t)head * 16 + w.rb) * 65536 + wid * 16384, (w.roleA && w.p < 7) ? 16384 : 0);
  LAS char* red = (LAS char*)lds + 45056;
  u32x4 qf[16]; u32x2 h0, h1; u32x4 pk[2], vv[2]; f32x4 kv[4];
#define QO(n) q_out1<n>(x, aq, qf, srdQo)
#define PH(dst, R) do { dst = pack4(phi4(aread4<R>())); PIN_V(dst); SBAR(); } while (0)
  X_POLL_ISSUE();
  *(LAS u32x4*)((LAS char*)lds + 163808) = u32x4{};
  QO(0); PH(h0, 16); QO(1); PH(h1, 20); pk[0] = cat(h0, h1); QO(2); PH(h0, 24); QO(3); PH(h1, 28); pk[1] = cat(h0, h1); QO(4);
  vv[0] = pack8(aread4<32>(), aread4<36>()); vv[1] = pack8(aread4<40>(), aread4<44>());
  if (r2 == 12) vv[1] = u32x4{0x3F803F80u, 0x3F803F80u, 0x3F803F80u, 0x3F803F80u};
  asm volatile("s_nop 1\n\tv_mfma_f32_16x16x32_bf16 %0, %4, %6, 0\n\tv_mfma_f32_16x16x32_bf16 %1, %5, %6, 0\n\tv_mfma_f32_16x16x32_bf16 %2, %4, %7, 0\n\tv_mfma_f32_16x16x32_bf16 %3, %5, %7, 0"
               : "=&v"(kv[0]), "=&v"(kv[1]), "=&v"(kv[2]), "=&v"(kv[3]) : "v"(pk[0]), "v"(pk[1]), "v"(vv[0]), "v"(vv[1]));
  SBAR();
  QO(5);
  asm volatile("s_waitcnt vmcnt(6)" ::: "memory");
  X_POLL_CHECK();
  k_first<0, 0>(x); PH(h0, 0); k_first<0, 1>(x); QO(6); k_first<0, 2>(x); PH(h1, 8); w.pq[0] = cat(h0, h1); k_first<0, 3>(x); QO(7);
  k_first<0, 4>(x); PH(h0, 4); k_first<0, 5>(x); QO(8); k_first<0, 6>(x); PH(h1, 12); w.pq[1] = cat(h0, h1); k_first<0, 7>(x); QO(9);
  if (g2 == 3) { w.pq[0][2] = 0u; w.pq[0][3] = 0u; w.pq[1][2] = 0u; w.pq[1][3] = 0u; }
  w.g[0] = __builtin_amdgcn_rcpf(1.f + __builtin_amdgcn_exp2f(-1.4426950408889634f * aread1<48>())); w.g[1] = __builtin_amdgcn_rcpf(1.f + __builtin_amdgcn_exp2f(-1.4426950408889634f * aread1<52>()));
  PIN_V(w.pq[0]); PIN_V(w.pq[1]); PIN_V(w.g[0]); PIN_V(w.g[1]);
  SBAR();
  QO(10);
#pragma unroll
  for (int q = 0; q < 4; ++q) { PIN_V(kv[q]); *(LAS f32x4*)(red + wid * 4096 + q * 1024 + x.voff) = kv[q]; }
  SBAR();
  QO(11); zero_O<0>(lds + 163808, make_iseq<8>{}); QO(12); zero_O<32>(lds + 163808, make_iseq<8>{}); QO(13); zero_O<64>(lds + 163808, make_iseq<8>{}); QO(14); zero_O<96>(lds + 163808, make_iseq<8>{}); QO(15);
  if (w.j >= 7) q_to_a<128>(qf, make_iseq<16>{});
  if (w.j < 7)  q_to_a<192>(qf, make_iseq<16>{});
  k_first<1, 0>(x); k_first<1, 1>(x); k_first<1, 2>(x); k_first<1, 3>(x); k_first<1, 4>(x); k_first<1, 5>(x); k_first<1, 6>(x); k_first<1, 7>(x);
}

constexpr float SM_BIAS = 8.f;
constexpr int KRING = 65536, VRING = 0;
constexpr int AO(int db, int qt) { return 8 * db + 4 * qt; }
constexpr int AQ(int qt, int s) { return 128 + 32 * qt + 4 * s; }
struct At {
  const LAS char* kb; const LAS char* vb; const LAS char* zp;
  unsigned voff, m0;
  rsrc_t srdK, srdV, srdP, srdF;
  u32x4 stp; u64 flt, tk; const char* ftp; char* sy; unsigned f3o; unsigned qlate;
  f32x4 S[2][4][2], negm[2], L[2], cm[4]; float rf;
  u32x4 P[2][2], Pn[2];
  bf16x8 pre[8], ones;
  unsigned orv[2];
  float mold[2], dd[8]; unsigned dp[4]; u32x4 dw;
  unsigned off0, off1, off2, j1, jd, w1; int i2, n1, n;
  u64 bad;
};
template <int PART> DI void next_off(At& c) {
  if constexpr (PART == 0) { c.off2 = c.off1 + 32768u; c.off2 = c.i2 == c.n1 ? c.j1 : c.off2; PIN_S(c.off2); }
  if constexpr (PART == 1) { c.off2 = c.i2 + 2 == c.n ? c.jd : c.off2; PIN_S(c.off2); }
  if constexpr (PART == 2) { c.off2 = c.i2 == c.n ? c.w1 : c.off2; PIN_S(c.off2); }
}
DI void rot_off(At& c) { c.off0 = c.off1; c.off1 = c.off2; c.i2 += 1; }
constexpr int own_read_at(int g) { return (g < 40 && ((g & 7) == 0 || (g & 7) == 2 || (g & 7) == 4 || (g & 7) == 6 || (g & 7) == 7)) ? 8 + (g >> 3) * 5 + ((g & 7) == 7 ? 4 : (g & 7) >> 1) : -1; }
constexpr int LE0[8] = {35, 39, 42, 44, 47, 51, 55, 59}, LE1[8] = {37, 41, 43, 45, 49, 53, 57, 61}, LCV[8] = {40, 48, 50, 52, 54, 56, 60, 63}, LOR[4] = {50, 58, 62, 63};
constexpr int exp0_gap(bool late, int k) { return late ? LE0[k] : k < 5 ? 8 * k + 1 : k == 5 ? 40 : k == 6 ? 42 : 49; }
constexpr int exp1_gap(bool late, int k) { return late ? LE1[k] : k < 5 ? 8 * k + 3 : k == 5 ? 41 : k == 6 ? 44 : 51; }
constexpr int cvt_gap(bool late, int k)  { return late ? LCV[k] : k < 5 ? 8 * k + 6 : k == 5 ? 43 : k == 6 ? 47 : 54; }
constexpr int or_gap(bool late, int k2)  { return late ? LOR[k2] : k2 == 0 ? 18 : k2 == 1 ? 34 : k2 == 2 ? 55 : 57; }
constexpr int kfrag_off(int f) { return ((2 * (f >> 4) + (f & 1)) * 8 + ((f >> 1) & 7)) * 1024; }
constexpr int vfrag_off(int f) { return f * 1024; }
template <int P, int GP, int LOFF, int G> DI void dma_at(const At& c, rsrc_t srd, unsigned soff, unsigned& so2) {
  if constexpr (G == GP - 1 && (P & 3) == 0) { m0_set<LOFF + P * 1024>(c.m0); if constexpr (P == 4) { so2 = soff + 4096; PIN_S(so2); } }
  if constexpr (G == GP) dma_go<true, (P & 3) * 1024>(P < 4 ? soff : so2, c.voff, srd);
}
constexpr int POS8[8] = {5, 13, 21, 29, 37, 45, 53, 61}, POSK[8] = {2, 5, 10, 13, 20, 23, 28, 31}, POSV[8] = {36, 39, 45, 48, 52, 56, 60, 63}, POS0[8] = {1, 5, 9, 13, 17, 21, 25, 29};
template <int TAB, int LOFF, int G, int... Ps> DI void dma_tab(const At& c, rsrc_t srd, unsigned soff, unsigned& so2, iseq<Ps...>) {
  (dma_at<Ps, (TAB == 0 ? POS8[Ps] : TAB == 1 ? POSK[Ps] : TAB == 2 ? POSV[Ps] : POS0[Ps]), LOFF, G>(c, srd, soff, so2), ...);
}
struct Ph { unsigned so2; bf16x8 f[32]; float e0[8], e1[8], th; };
template <int X, int HF, bool LATE, int G, int K> DI void sm_fill1(At& c, u32x4 (&dst)[2], Ph& h) {
  constexpr int qt = K >> 2, m = 2 * HF + ((K >> 1) & 1), e = 2 * (K & 1);
  if constexpr (G == exp0_gap(LATE, K)) asm volatile("v_exp_f32 %0, %1" : "=v"(h.e0[K]) : "v"(c.S[X][m][qt][e]));
  if constexpr (G == exp1_gap(LATE, K)) asm volatile("v_exp_f32 %0, %1" : "=v"(h.e1[K]) : "v"(c.S[X][m][qt][e + 1]));
  if constexpr (G == cvt_gap(LATE, K)) dst[qt][K & 3] = cvt_asm(h.e0[K], h.e1[K]);
}
template <int X, int HF, bool LATE, int G, int K> DI void sm_or1(At& c, u32x4 (&dst)[2]) {
  if constexpr (K < 4) { if constexpr (G == or_gap(LATE, K)) {
    if constexpr (HF == 0 && (K & 1) == 0) asm volatile("v_or_b32 %0, %1, %2" : "=v"(c.orv[K >> 1]) : "v"(dst[K >> 1][0]), "v"(dst[K >> 1][1]));
    else asm volatile("v_or3_b32 %0, %1, %2, %0" : "+v"(c.orv[K >> 1]) : "v"(dst[K >> 1][2 * (K & 1)]), "v"(dst[K >> 1][2 * (K & 1) + 1]));
  } }
}
template <int X, int HF, bool LATE, int G, int... Ks> DI void sm_fill(At& c, u32x4 (&dst)[2], Ph& h, iseq<Ks...>) { (sm_fill1<X, HF, LATE, G, Ks>(c, dst, h), ...); (sm_or1<X, HF, LATE, G, Ks>(c, dst), ...); }
template <int X, int G> DI void sm_fill_q1(At& c, u32x4 (&dst)[2], Ph& h) {
  sm_fill1<X, 0, false, G, 4>(c, dst, h); sm_fill1<X, 0, false, G, 5>(c, dst, h); sm_fill1<X, 0, false, G, 6>(c, dst, h); sm_fill1<X, 0, false, G, 7>(c, dst, h);
  sm_or1<X, 0, false, G, 2>(c, dst); sm_or1<X, 0, false, G, 3>(c, dst);
}
template <int G> DI void frag_wait() { if constexpr ((G & 7) == 0) AFENCE(); if constexpr ((G & 7) == 0 && G <= 40) LGKM(G >= 32 ? 8 : 4 + (G >> 3)); }
constexpr int CMG[32] = {2, 4, 5, 7, 10, 12, 13, 15, 20, 21, 23, 26, 28, 29, 31, 36, 37, 39, 39, 45, 48, 50, 52, 53, 56, 58, 59, 59, 60, 61, 62, 63};
template <int QT, int G, int I> DI void cm_op(At& c, Ph& h) {
  if constexpr (CMG[I] == G) { constexpr int m = I >> 3, e = (I >> 1) & 3;
    if constexpr ((I & 1) == 0) { h.th = c.rf + (0.5f - (16 * m + e)) * 1e36f; PIN_V(h.th); }
    else { float v; asm volatile("v_min_f32 %0, %1, %2" : "=v"(v) : "v"(h.th), "v"(c.negm[QT][0])); c.cm[m][e] = v; } }
}
template <int QT, int G, int... Is> DI void cm_fill(At& c, Ph& h, iseq<Is...>) { (cm_op<QT, G, Is>(c, h), ...); }
template <int M> DI void drain_op(At& c) {
  constexpr int n = M / 15, k = M % 15, s2 = n >> 1, q2 = n & 1, R0 = AO(2 * s2, q2), R1 = AO(2 * s2 + 1, q2);
  if constexpr (k < 4) c.dd[k] = aread1<R0 + k>();
  else if constexpr (k == 4) { if constexpr (n > 0) KEEP(c.dw); c.dp[0] = cvt_asm(c.dd[0], c.dd[1]); }
  else if constexpr (k == 5) lds_to_a<R0, 0>(c.zp);
  else if constexpr (k == 6 || k == 7) c.dd[k - 2] = aread1<R1 + k - 6>();
  else if constexpr (k == 8) c.dp[1] = cvt_asm(c.dd[2], c.dd[3]);
  else if constexpr (k == 9 || k == 10) c.dd[k - 3] = aread1<R1 + k - 7>();
  else if constexpr (k == 11) c.dp[2] = cvt_asm(c.dd[4], c.dd[5]);
  else if constexpr (k == 12) lds_to_a<R1, 0>(c.zp);
  else if constexpr (k == 13) c.dp[3] = cvt_asm(c.dd[6], c.dd[7]);
  else { c.dw = u32x4{c.dp[0], c.dp[1], c.dp[2], c.dp[3]}; if constexpr (n < 15) st16_sc1(c.dw, c.voff + (n & 3) * 1024, c.srdP, (n >> 2) * 4096); else st16_pad(c.dw, c.voff + (n & 3) * 1024, c.srdP, (n >> 2) * 4096); }
}
constexpr int drain_upto(int t) { return t >= 60 ? 240 : t * 4; }
template <int T> DI void drain_gap(At& c) {
  constexpr int lo = drain_upto(T), hi = drain_upto(T + 1);
  if constexpr (lo < hi) { drain_op<lo>(c); drain_op<lo + 1>(c); drain_op<lo + 2>(c); drain_op<lo + 3>(c); }
}
template <int X, int KIND, int NV, int G> DI void gapA(At& c, Ph& h) {
  const unsigned vsoff = c.off0, ksoff = c.off1;
  frag_wait<G>();
  if constexpr (KIND == 1 && G > 0 && G < 32 && (G & 7) == 0)
    asm volatile("s_cmp_gt_u32 %0, 6\n\ts_cbranch_scc1 1f\n\ts_waitcnt vmcnt(%1)\n1:" :: "s"(c.qlate), "i"(13 - (G >> 1) + (G >> 2)) : "scc", "memory");
  constexpr int fi = G >> 1, qt = G & 1, s = (fi >> 1) & 7, m = 2 * (fi >> 4) + (fi & 1), rd = own_read_at(G);
  constexpr bool masked = ((KIND == 4 || KIND == 5) && qt == 0) || ((KIND == 3 || KIND == 6) && qt == 1);
  if constexpr (KIND != 6 || qt == 1) {
    if constexpr (s == 0) asm volatile("v_mfma_f32_16x16x32_bf16 %0, %1, a[%c3:%c4], %2" : "=&v"(c.S[X][m][qt]) : "v"(h.f[fi]), "v"(masked ? c.cm[m] : c.negm[qt]), "i"(AQ(qt, 0)), "i"(AQ(qt, 0) + 3));
    else                  asm volatile("v_mfma_f32_16x16x32_bf16 %0, %1, a[%c2:%c3], %0" : "+v"(c.S[X][m][qt]) : "v"(h.f[fi]), "i"(AQ(qt, s)), "i"(AQ(qt, s) + 3));
  }
  if constexpr (masked && s == 1) KEEP(c.cm[m]);
  if constexpr (rd >= 0 && rd < 32) h.f[rd] = ld128(c.kb + X * 32768 + kfrag_off(rd));
  if constexpr (KIND == 2) dma_tab<1, KRING + (X ^ 1) * 32768, G>(c, c.srdK, ksoff, h.so2, make_iseq<8>{});
  dma_tab<(KIND == 2 ? 2 : KIND == 1 ? 3 : 0), VRING + X * 32768, G>(c, c.srdV, vsoff, h.so2, make_iseq<8>{});
  if constexpr (KIND == 1) sm_fill<X, 0, true, G>(c, c.P[0], h, make_iseq<8>{});
  else sm_fill<X ^ 1, 1, false, G>(c, c.P[1], h, make_iseq<8>{});
  if constexpr (KIND == 3) drain_gap<24 + G>(c);
  if constexpr (KIND == 3 && G == 63) { asm volatile("s_waitcnt vmcnt(4)" ::: "memory"); flag_set0(c.sy, c.f3o, c.tk, c.voff); }
  if constexpr (X == 1 && (KIND == 0 || KIND == 6) && G == 2) asm volatile("global_load_dwordx2 %0, %1, off sc1" : "+v"(c.flt) : "v"(c.ftp) : "memory");
  if constexpr (KIND < 5) {
    if constexpr (G == (KIND == 1 ? 11 : KIND == 2 ? 29 : 39)) next_off<0>(c);
    if constexpr (G == (KIND == 1 ? 19 : KIND == 2 ? 37 : 50)) next_off<1>(c);
    if constexpr (KIND == 4 && G == 52) next_off<2>(c);
  }
  if constexpr (KIND == 5 && G == 39) c.off2 = c.w1;
  if constexpr (G == 59 && KIND != 1) {
    if constexpr (KIND == 2 || KIND == 3) c.bad = __builtin_amdgcn_ballot_w64(((c.orv[0] | c.orv[1]) & 0x40004000u) != 0u || (c.orv[0] & 0x60006000u) == 0u || (c.orv[1] & 0x60006000u) == 0u);
    else c.bad = __builtin_amdgcn_ballot_w64(((c.orv[0] | c.orv[1]) & 0x40004000u) != 0u);
    PIN_S(c.bad);
  }
  if constexpr (G == 46) { LGKM(0); asm volatile("s_waitcnt vmcnt(%0)\n\ts_barrier" :: "i"(NV) : "memory"); }
  if constexpr (G >= 48 && (G & 1) == 0) {
    if constexpr (KIND == 1) c.pre[(G - 48) >> 1] = ld128(c.kb + (X ^ 1) * 32768 + kfrag_off((G - 48) >> 1));
    else                     c.pre[(G - 48) >> 1] = ld128(c.vb + (X ^ 1) * 32768 + vfrag_off((G - 48) >> 1));
  }
  SBAR();
}
template <int X, int KIND, int NV, int... Gs> DI bool phaseA_(At& c, iseq<Gs...>) {
  Ph h;
#pragma unroll
  for (int i = 0; i < 8; ++i) h.f[i] = c.pre[i];
  (gapA<X, KIND, NV, Gs>(c, h), ...);
  return KIND != 1 && c.bad != 0ull;
}
template <int X, int KIND, int NV> DI bool phaseA(At& c) { return phaseA_<X, KIND, NV>(c, make_iseq<64>{}); }
template <int D, int S> DI void amov() { asm volatile("v_accvgpr_mov_b32 a[%c0], a[%c1]" :: "i"(D), "i"(S)); }
template <int R> DI void load1a(rsrc_t srd, unsigned voff, unsigned soff) { asm volatile("buffer_load_dwordx4 a[%c3:%c4], %0, %1, %2 offen sc1" :: "v"(voff), "s"(srd), "s"(soff), "i"(R), "i"(R + 3) : "memory"); }
template <int R, int IMM> DI void load1ai(rsrc_t srd, unsigned voff, unsigned soff) { asm volatile("buffer_load_dwordx4 a[%c3:%c4], %0, %1, %2 offen offset:%5 sc1" :: "v"(voff), "s"(srd), "s"(soff), "i"(R), "i"(R + 3), "i"(IMM) : "memory"); }
template <int N> DI void aux_load(At& c) {
  if constexpr (N == 0) asm volatile("buffer_load_dwordx4 %0, %1, %2, %3 offen sc1" : "=v"(c.stp) : "v"(c.voff), "s"(c.srdP), "s"(16384u) : "memory");
  else if constexpr (N <= 16) load1ai<192 + 32 * ((N - 1) & 1) + 4 * ((N - 1) >> 1), ((N - 1) & 3) * 1024>(c.srdP, c.voff, ((N - 1) >> 2) * 4096);
  else load1ai<128 + 4 * (N - 17), (N - 17) * 1024>(c.srdF, c.voff, 0u);
}
constexpr int AUXG[19] = {2, 5, 7, 10, 13, 15, 20, 21, 23, 28, 29, 31, 36, 37, 39, 45, 48, 52, 53};
constexpr int aux_at(int g) { for (int i = 0; i < 19; ++i) if (AUXG[i] == g) return i; return -1; }
constexpr int vdrain_off(int f) { return 32768 + ((f & 1) * 16 + (f >> 1)) * 1024; }
template <int X, int KIND, int CMQ, int G> DI void gapB(At& c, Ph& h, unsigned ksoff) {
  frag_wait<G>();
  constexpr int fi = G >> 1, qt = G & 1, ks = fi >> 4, db = fi & 15, rd = own_read_at(G);
  mma_acc<AO(db, qt)>(h.f[fi], c.P[ks][qt]);
  if constexpr (rd >= 0 && rd < 32) h.f[rd] = ld128(c.vb + (X ^ 1) * 32768 + vfrag_off(rd));
  if constexpr (KIND != 3) dma_tab<0, KRING + X * 32768, G>(c, c.srdK, ksoff, h.so2, make_iseq<8>{});
  if constexpr (KIND == 3) sm_fill_q1<X, G>(c, c.Pn, h); else sm_fill<X, 0, false, G>(c, c.Pn, h, make_iseq<8>{});
  if constexpr (CMQ >= 0) cm_fill<CMQ, G>(c, h, make_iseq<32>{});
  if constexpr (G == 46) { LGKM(0); asm volatile("s_waitcnt vmcnt(%0)\n\ts_barrier" :: "i"(KIND == 3 ? 15 : 14) : "memory"); }
  if constexpr (KIND == 3 && aux_at(G) >= 0) aux_load<aux_at(G)>(c);
  if constexpr (KIND == 2) amov<128 + G, 192 + G>();
  if constexpr (KIND == 4 && G >= 40) drain_gap<G - 40>(c);
  if constexpr (G >= 48 && (G & 1) == 0) {
    if constexpr (KIND == 3) c.pre[(G - 48) >> 1] = ld128(c.vb + vdrain_off((G - 48) >> 1));
    else                     c.pre[(G - 48) >> 1] = ld128(c.kb + (X ^ 1) * 32768 + kfrag_off((G - 48) >> 1));
  }
  SBAR();
}
template <int X, int KIND, int CMQ, int... Gs> DI void phaseB_(At& c, unsigned ksoff, iseq<Gs...>) {
  Ph h;
#pragma unroll
  for (int i = 0; i < 8; ++i) h.f[i] = c.pre[i];
  MMA_V(c.L[0], c.ones, c.P[0][0]); SBAR(); MMA_V(c.L[1], c.ones, c.P[0][1]); SBAR();
  MMA_V(c.L[0], c.ones, c.P[1][0]); SBAR(); MMA_V(c.L[1], c.ones, c.P[1][1]); SBAR();
  (gapB<X, KIND, CMQ, Gs>(c, h, ksoff), ...);
  if constexpr (KIND != 3) c.P[0][0] = c.Pn[0];
  c.P[0][1] = c.Pn[1];
  PIN_V(c.L[0]); PIN_V(c.L[1]);
}
template <int X, int KIND, int CMQ = -1> DI void phaseB(At& c) { SBAR(); phaseB_<X, KIND, CMQ>(c, c.off2, make_iseq<64>{}); rot_off(c); }
template <int X> DI float rowmax(At& c, int qt) {
  float mx = fmaxf(fmaxf(c.S[X][0][qt][0], c.S[X][0][qt][1]), fmaxf(c.S[X][0][qt][2], c.S[X][0][qt][3]));
#pragma unroll
  for (int m = 1; m < 4; ++m) mx = fmaxf(mx, fmaxf(fmaxf(c.S[X][m][qt][0], c.S[X][m][qt][1]), fmaxf(c.S[X][m][qt][2], c.S[X][m][qt][3])));
  mx = fmaxf(mx, __shfl_xor(mx, 16)); mx = fmaxf(mx, __shfl_xor(mx, 32));
  return mx;
}
DI f32x4 exp4(const f32x4& v) { f32x4 o; o[0] = __builtin_amdgcn_exp2f(v[0]); o[1] = __builtin_amdgcn_exp2f(v[1]); o[2] = __builtin_amdgcn_exp2f(v[2]); o[3] = __builtin_amdgcn_exp2f(v[3]); return o; }
template <int QT, int... Ds> DI void scaleO(float f, iseq<Ds...>) { (awrite4<AO(Ds, QT)>(aread4<AO(Ds, QT)>() * f), ...); }
template <int X, int Q0> DI void pin_all(At& c) {
#pragma unroll
  for (int m = 0; m < 4; ++m)
#pragma unroll
    for (int qt = Q0; qt < 2; ++qt) PIN_V(c.S[X][m][qt]);
}
template <int XP, int MODE, int Q0 = 0, int N0 = 0> DI void rescale(At& c) {
  if constexpr (MODE == 1) {
    u64 b0 = __builtin_amdgcn_ballot_w64((c.orv[0] & 0x60006000u) != 0u), b1 = __builtin_amdgcn_ballot_w64((c.orv[1] & 0x60006000u) != 0u);
    b0 |= b0 >> 32; b1 |= b1 >> 32; b0 |= b0 >> 16; b1 |= b1 >> 16;
    if ((b0 & b1 & 0xffffull) == 0xffffull) c.bad = __builtin_amdgcn_ballot_w64(((c.orv[0] | c.orv[1]) & 0x40004000u) != 0u);
  }
  if (MODE != 1 || c.bad != 0ull) {
    asm volatile("s_nop 7\n\ts_nop 1" ::: "memory");
    pin_all<XP, Q0>(c); if constexpr (MODE != 2) pin_all<XP ^ 1, N0>(c);
#pragma unroll
    for (int qt = Q0; qt < 2; ++qt) {
      const float mx = rowmax<XP>(c, qt);
      const float dl = MODE == 1 ? ((mx >= 0.5f || mx < -60.f) ? mx + SM_BIAS : 0.f) : fmaxf(mx + SM_BIAS, 0.f);
#pragma unroll
      for (int m = 0; m < 4; ++m) { c.S[XP][m][qt] -= dl; if (MODE != 2 && qt >= N0) c.S[XP ^ 1][m][qt] -= dl; }
      if constexpr (MODE == 2) c.mold[qt] += dl; else { c.negm[qt] -= dl; PIN_V(c.negm[qt]); }
      if constexpr (MODE != 1) { const float fct = __builtin_amdgcn_exp2f(-dl);
        if (qt == 0) scaleO<0>(fct, make_iseq<16>{}); else scaleO<1>(fct, make_iseq<16>{});
        c.L[qt] *= fct; PIN_V(c.L[qt]); }
      c.P[0][qt] = pack8(exp4(c.S[XP][0][qt]), exp4(c.S[XP][1][qt])); c.P[1][qt] = pack8(exp4(c.S[XP][2][qt]), exp4(c.S[XP][3][qt]));
      PIN_V(c.P[0][qt]); PIN_V(c.P[1][qt]);
    }
    if constexpr (MODE != 2) pin_all<XP ^ 1, N0>(c);
  }
  asm volatile("s_nop 1" ::: "memory");
}
template <int... Ns> DI void q_loads(rsrc_t srd, unsigned voff, unsigned soff, iseq<Ns...>) { (load1a<AQ(Ns & 1, Ns >> 1)>(srd, voff, soff + (Ns & 1) * 8192 + (Ns >> 1) * 1024), ...); }
template <int G> DI void merge_mma1(const bf16x8& se, const bf16x8& so) {
  constexpr int db = G >> 1, qt = G & 1, R = AO(db, qt), B = 192 + 32 * qt + 4 * (db >> 1);
  asm volatile("v_mfma_f32_16x16x32_bf16 a[%c1:%c2], %0, a[%c3:%c4], a[%c1:%c2]" :: "v"((db & 1) ? so : se), "i"(R), "i"(R + 3), "i"(B), "i"(B + 3));
}
template <int QT, int... Ss> DI void scale_part(float f, iseq<Ss...>) {
  (([&] { const u32x4 pb = __builtin_bit_cast(u32x4, aread4<192 + 32 * QT + 4 * Ss>());
          awrite4<192 + 32 * QT + 4 * Ss>(__builtin_bit_cast(f32x4, pack8(f32x4{bflo(pb[0]), bfhi(pb[0]), bflo(pb[1]), bfhi(pb[1])} * f, f32x4{bflo(pb[2]), bfhi(pb[2]), bflo(pb[3]), bfhi(pb[3])} * f))); }()), ...);
}
DI void phase23(Wg& w, u16* out) {
  Op o;
  wg_fresh(w);
  unsigned lane = w.lane; PIN_V(lane);
  const unsigned wid = w.wid, r = lane & 15, g = lane >> 4;
  const int head = w.head, hi = 15 - w.p;
  const bool split = w.p < 7, merge = w.roleA && split, two = !w.roleA && split;
  char* sy = w.ws + WS_SYNC;
  At c;
  c.kb = w.lds + KRING + lane * 16; c.vb = w.lds + VRING + lane * 16; PIN_V(c.kb); PIN_V(c.vb);
  c.voff = lane * 16; c.m0 = w.lds0 + wid * 8192;
  c.srdK = mk_rsrc(w.ws + WS_K + (size_t)head * (1u << 20), (unsigned)(WS_END - WS_K) - head * (1u << 20)); c.srdV = mk_rsrc(w.ws + WS_V + (size_t)head * (1u << 20), 1u << 20);
  c.srdP = mk_rsrc(w.ws + WS_PART + (size_t)((head * 8 + w.p) * 4 + wid) * 17408, 17408);
  c.ones = __builtin_bit_cast(bf16x8, u32x4{0x3F803F80u, 0x3F803F80u, 0x3F803F80u, 0x3F803F80u}); PIN_V(c.ones);
  c.negm[0] = f32x4{-SM_BIAS, -SM_BIAS, -SM_BIAS, -SM_BIAS}; c.negm[1] = c.negm[0]; PIN_V(c.negm[0]); PIN_V(c.negm[1]);
  c.L[0] = f32x4{}; c.L[1] = f32x4{}; PIN_V(c.L[0]); PIN_V(c.L[1]);
  c.zp = w.lds + 163808; PIN_V(c.zp);
  const unsigned wofs = wid * 8192; const int nseam = two ? 14 - 2 * w.p : -1;
  c.n = w.roleA ? 18 : 16; c.n1 = w.roleA ? 16 - 2 * w.p : nseam; c.i2 = 2;
  c.off0 = (w.roleA ? 14 * 32768u : 0u) + wofs; c.off1 = c.off0 + 32768u; c.j1 = (w.roleA ? (14 - 2 * w.p) * 32768u : 0u) + wofs; c.jd = w.rb * 65536u + wofs;
  c.w1 = (unsigned)(WS_W - WS_K) + head * (WIMG - (1u << 20)) + wofs;
  const int ired = w.j == 7 ? 7 : -1;
  { const u64 pf = (u64)sy + ((lane < 4 || !merge) ? fl_off(head, 7, lane & 3, F_SUM) : fl_off(head, w.p, wid, F_PART)), pc = (u64)w.code + code_line((blockIdx.x >> 3) * 4 + wid, lane - 40);
    c.ftp = (const char*)(lane < 40 ? pf : pc); }
  unsigned o0, o1;
  { const unsigned one = (r >> 2) == g ? ((r & 1) ? 0x3F800000u : 0x3F80u) : 0u; o0 = (r & 2) ? 0u : one; o1 = (r & 2) ? one : 0u; }
  bf16x8 se = {o0, o1, 0u, 0u}, so = {0u, 0u, o0, o1}; PIN_V(se); PIN_V(so);
  c.flt = 0; c.sy = sy; c.tk = w.tkH; c.f3o = fl_off(head, w.j, wid, F_PART); c.qlate = (unsigned)w.j;
  asm volatile("s_waitcnt vmcnt(8)" ::: "memory");
  flag_set1(sy, fl_off(head, w.j, wid, F_Q), w.tkH, (0x7f00u >> w.j & 1u) ^ 1u, c.voff);
  if (two) {
    flag_wait(sy + fl_off(head, 8 + w.p, wid, F_Q), true, w.tkH);
    q_loads(mk_rsrc(w.ws + WS_Q + ((size_t)head * 16 + hi) * 65536, 65536), c.voff, wid * 16384, make_iseq<16>{});
  }
  asm volatile("s_waitcnt lgkmcnt(0)\n\ts_barrier" ::: ACLOB, "memory");
#pragma unroll
  for (int j = 0; j < 8; ++j) c.pre[j] = ld128(c.kb + kfrag_off(j));
  { const LAS char* red = w.lds + 45056 + wid * 1024 + c.voff;
    f32x4 sum = *(const LAS f32x4*)red;
#pragma unroll
    for (int q = 1; q < 4; ++q) sum += *(const LAS f32x4*)(red + q * 4096);
    st16_pad(__builtin_bit_cast(u32x4, sum), c.voff, mk_rsrc(w.ws + WS_KVP + ((size_t)head * 16 + w.rb) * 4096, 4096), wid * 1024); }
  asm volatile("s_waitcnt vmcnt(13)" ::: ACLOB, "memory");
  SBAR();
  { phaseA<0, 1, 8>(c);
    flag_set1(sy, fl_off(head, w.j, 0, F_LIN), w.tkH, wid, c.voff);
    rot_off(c);
    SBAR(); }
  if (__builtin_expect(phaseA<1, 2, 11>(c), 0)) rescale<0, 1>(c);
  int lim = two ? nseam : c.n - 2, more;
  const float rfd = (float)((int)wid * 16 + (int)r - 4 * (int)g) * 1e36f;
  do {
    while (c.i2 <= lim) {
      phaseB<1, 0>(c);
      if (__builtin_expect(phaseA<0, 0, 14>(c), 0)) rescale<1, 0>(c);
      phaseB<0, 0>(c);
      if (__builtin_expect(phaseA<1, 0, 14>(c), 0)) rescale<0, 0>(c);
      if (__builtin_expect(c.i2 == c.n - 3, 0)) {
        const u64 pw = (u64)w.ws + WS_W + head * WIMG + ((w.j * 4 + wid) + (lane - 8) * 64) * 128;
        c.ftp = (lane >= 8 && lane < 40) ? (const char*)pw : c.ftp; PIN_V(c.ftp);
        asm volatile("s_nop 1" ::: "memory");
      }
      if (__builtin_expect(c.i2 == ired, 0)) {
        flag_wait(sy + fl_off(head, r, 0, F_LIN), true, w.tkH);
        const rsrc_t sk = mk_rsrc(w.ws + WS_KVP + (size_t)head * 65536, 65536);
        f32x4 sum = __builtin_bit_cast(f32x4, __builtin_amdgcn_raw_buffer_load_b128(sk, c.voff, wid * 1024, 16));
#pragma unroll
        for (int jj = 1; jj < 16; ++jj) sum += __builtin_bit_cast(f32x4, __builtin_amdgcn_raw_buffer_load_b128(sk, c.voff, wid * 1024 + jj * 4096, 16));
        __builtin_amdgcn_raw_buffer_store_b64(u32x2{cvtpk(sum[0], sum[1]), cvtpk(sum[2], sum[3])}, mk_rsrc(w.ws + WS_KVF + head * 2048, 2048), c.voff, (wid >> 1) * 1024 + (wid & 1) * 8, 16);
        asm volatile("s_waitcnt vmcnt(0)" ::: "memory");
        flag_set0(sy, fl_off(head, 7, wid, F_SUM), w.tkH, c.voff);
        asm volatile("s_nop 1" ::: "memory");
      }
    }
    more = c.i2 - 1 - nseam;
    if (more == 0) {
      c.mold[0] = -c.negm[0][0]; c.mold[1] = -c.negm[1][0];
      c.negm[0] = f32x4{-SM_BIAS, -SM_BIAS, -SM_BIAS, -SM_BIAS}; c.negm[1] = c.negm[0]; PIN_V(c.negm[0]); PIN_V(c.negm[1]);
      c.rf = w.p == 0 ? rfd : 1e38f; PIN_V(c.rf);
      asm volatile("s_nop 1" ::: "memory");
      phaseB<1, 2, 0>(c);
      asm volatile("s_nop 1" ::: ACLOB);
      if (__builtin_expect(phaseA<0, 4, 14>(c), 0)) rescale<1, 2>(c);
      asm volatile("s_nop 1" ::: "memory");
      phaseB<0, 4, 1>(c);
      st16_pad(__builtin_bit_cast(u32x4, f32x4{c.mold[0], c.L[0][0], c.mold[1], c.L[1][0]}), c.voff, c.srdP, 16384);
      c.L[0] = f32x4{}; c.L[1] = f32x4{}; PIN_V(c.L[0]); PIN_V(c.L[1]);
      if (__builtin_expect(phaseA<1, 3, 30>(c), 0)) rescale<0, 1>(c);
      asm volatile("s_nop 1" ::: "memory");
      lim = c.n - 2;
    }
    PIN_S(more);
  } while (more == 0);
  if (c.i2 <= c.n) {
    c.rf = rfd; PIN_V(c.rf);
    asm volatile("s_nop 1" ::: "memory");
    phaseB<1, 0, 0>(c);
    asm volatile("s_nop 1" ::: "memory");
    if (__builtin_expect(phaseA<0, 5, 14>(c), 0)) rescale<1, 0>(c);
    asm volatile("s_nop 1" ::: "memory");
    phaseB<0, 0, 1>(c);
    asm volatile("s_nop 1" ::: "memory");
    if (__builtin_expect(phaseA<1, 6, 14>(c), 0)) rescale<0, 0, 0, 1>(c);
  }
  c.mold[0] = -c.negm[0][0]; c.mold[1] = -c.negm[1][0];
  asm volatile("s_waitcnt vmcnt(8)" : "+v"(c.flt) :: "memory");
  if (__builtin_amdgcn_ballot_w64(lane < 8 && !flag_ok(c.flt, w.tkH)) != 0ull) flag_wait(c.ftp, lane < 8, w.tkH);
  c.srdF = mk_rsrc(w.ws + WS_KVF + head * 2048, 2048);
  if (!merge) c.srdP = mk_rsrc(w.ws, 0);
  asm volatile("s_nop 1" ::: "memory");
  phaseB<1, 3>(c);
  PIN_V(c.stp);
  wg_fresh(w); op_init(o, w);
  { const float mB0 = __uint_as_float(c.stp[0]), mB1 = __uint_as_float(c.stp[2]); float lB0 = __uint_as_float(c.stp[1]), lB1 = __uint_as_float(c.stp[3]);
    if (__builtin_expect(merge && __builtin_amdgcn_ballot_w64(mB0 != c.mold[0] || mB1 != c.mold[1]) != 0ull, 0)) {
      asm volatile("s_waitcnt vmcnt(0)\n\ts_nop 7" ::: "memory");
      const float d0 = fmaxf(mB0 - c.mold[0], 0.f), d1 = fmaxf(mB1 - c.mold[1], 0.f);
      c.mold[0] += d0; c.mold[1] += d1;
      const float fa0 = __builtin_amdgcn_exp2f(-d0), fa1 = __builtin_amdgcn_exp2f(-d1), fb0 = __builtin_amdgcn_exp2f(mB0 - c.mold[0]), fb1 = __builtin_amdgcn_exp2f(mB1 - c.mold[1]);
      scaleO<0>(fa0, make_iseq<16>{}); scaleO<1>(fa1, make_iseq<16>{}); scale_part<0>(fb0, make_iseq<8>{}); scale_part<1>(fb1, make_iseq<8>{});
      c.L[0] *= fa0; c.L[1] *= fa1; lB0 *= fb0; lB1 *= fb1;
#pragma unroll
      for (int m = 0; m < 4; ++m) c.S[1][m][1] -= d1;
      c.P[0][1] = pack8(exp4(c.S[1][0][1]), exp4(c.S[1][1][1])); c.orv[1] = c.P[0][1][0] | c.P[0][1][1] | c.P[0][1][2] | c.P[0][1][3];
    }
    c.L[0] += lB0; c.L[1] += lB1; PIN_V(c.L[0]); PIN_V(c.L[1]); }
  asm volatile("s_nop 1" ::: "memory");
  u32x4 cf[2][9]; f32x4 acc[2][4][2], ln[4]; float den0, den1; u64 fl5;
  const char* f5p = w.ws + WS_SYNC + fl_off(r, w.j, w.wid, F_OUT + ((lane & 16) ? (o.u0 < 8 ? 1 : 2) : 0));
  unsigned pending = o.u0 < 12 ? 0xffffffffu : 0xffffu;
  fl5 = 0;
#define T_LIN_A() do { PIN_V(ln[0]); PIN_V(ln[1]); PIN_V(ln[2]); PIN_V(ln[3]); den0 = __shfl(ln[1][0], 48 + r); den1 = __shfl(ln[3][0], 48 + r); } while (0)
#define T_LIN_B() do { const float og0 = (1.f - w.g[0]) * __builtin_amdgcn_rcpf(den0 + 1e-6f), og1 = (1.f - w.g[1]) * __builtin_amdgcn_rcpf(den1 + 1e-6f); \
    cf[0][8] = pack8(ln[0] * og0, ln[1] * og0); cf[1][8] = pack8(ln[2] * og1, ln[3] * og1); if (g == 3) { cf[0][8][2] = 0u; cf[0][8][3] = 0u; cf[1][8][2] = 0u; cf[1][8][3] = 0u; } \
    PIN_V(cf[0][8]); PIN_V(cf[1][8]); } while (0)
#define O_PUBLISH(rnd) flag_set0(w.ws + WS_SYNC, fl_off(w.head, w.j, w.wid, F_OUT + (rnd)), w.tkR, c.voff)
#define O_POLL_ISSUE(m) asm volatile("s_mov_b64 exec, %2\n\tglobal_load_dwordx2 %0, %1, off sc1\n\ts_mov_b64 exec, -1" : "+v"(fl5) : "v"(f5p), "s"((u64)(m)) : "memory")
#define POLLSLEEP 8
#define O_LAST(nv) static_assert(nv == 0, "")
  SBAR();
  const bf16x8 vf0 = c.pre[0]; const bf16x8 vf1 = c.pre[1]; const bf16x8 vf2 = c.pre[2]; const bf16x8 vf3 = c.pre[3]; const bf16x8 vf4 = c.pre[4]; const bf16x8 vf5 = c.pre[5]; const bf16x8 vf6 = c.pre[6]; const bf16x8 vf7 = c.pre[7]; float sc0 = w.g[0] * __builtin_amdgcn_rcpf(c.L[0][0]); PIN_V(sc0); asm volatile("s_waitcnt vmcnt(14)" ::: ACLOB, "memory"); merge_mma1<0>(se, so); const float te0 = exp_asm(c.S[1][2][1][0]); SBAR();
  merge_mma1<1>(se, so); const float te1 = exp_asm(c.S[1][2][1][1]); const unsigned sw1_0 = dma_prep<98304, 32768>(c.m0); SBAR();
  merge_mma1<2>(se, so); const float te2 = exp_asm(c.S[1][2][1][2]); SBAR();
  merge_mma1<3>(se, so); const float te3 = exp_asm(c.S[1][2][1][3]); dma_go<true, 0>(sw1_0, c.voff, o.srdW); SBAR();
  merge_mma1<4>(se, so); const float te4 = exp_asm(c.S[1][3][1][0]); SBAR();
  merge_mma1<5>(se, so); const float te5 = exp_asm(c.S[1][3][1][1]); SBAR();
  merge_mma1<6>(se, so); const float te6 = exp_asm(c.S[1][3][1][2]); SBAR();
  merge_mma1<7>(se, so); const float te7 = exp_asm(c.S[1][3][1][3]); dma_go<true, 1024>(sw1_0, c.voff, o.srdW); const float ea0_0_0 = aread1<0>(); SBAR();
  asm volatile("s_waitcnt vmcnt(12)" ::: ACLOB, "memory"); merge_mma1<8>(se, so); c.P[1][1][0] = cvt_asm(te0, te1); const float eb0_0_0 = aread1<1>(); const float ea0_0_1 = aread1<2>(); SBAR();
  merge_mma1<9>(se, so); c.P[1][1][1] = cvt_asm(te2, te3); const float eb0_0_1 = aread1<3>(); const float ea0_0_2 = aread1<8>(); const float eb0_0_2 = aread1<9>(); SBAR();
  merge_mma1<10>(se, so); c.P[1][1][2] = cvt_asm(te4, te5); const float ea0_0_3 = aread1<10>(); const float eb0_0_3 = aread1<11>(); const float fa0_0_0 = mul_asm(ea0_0_0, sc0); SBAR();
  merge_mma1<11>(se, so); c.P[1][1][3] = cvt_asm(te6, te7); dma_go<true, 2048>(sw1_0, c.voff, o.srdW); const float fb0_0_0 = mul_asm(eb0_0_0, sc0); const float fa0_0_1 = mul_asm(ea0_0_1, sc0); SBAR();
  merge_mma1<12>(se, so); const float fb0_0_1 = mul_asm(eb0_0_1, sc0); const float fa0_0_2 = mul_asm(ea0_0_2, sc0); const float fb0_0_2 = mul_asm(eb0_0_2, sc0); const float fa0_0_3 = mul_asm(ea0_0_3, sc0); SBAR();
  merge_mma1<13>(se, so); c.orv[1] |= c.P[1][1][0] | c.P[1][1][1]; c.orv[1] |= c.P[1][1][2] | c.P[1][1][3]; PIN_V(c.orv[1]); const float fb0_0_3 = mul_asm(eb0_0_3, sc0); cf[0][0][0] = cvt_asm(fa0_0_0, fb0_0_0); SBAR();
  merge_mma1<14>(se, so); cf[0][0][1] = cvt_asm(fa0_0_1, fb0_0_1); cf[0][0][2] = cvt_asm(fa0_0_2, fb0_0_2); cf[0][0][3] = cvt_asm(fa0_0_3, fb0_0_3); const float ea1_0_0 = aread1<16>(); SBAR();
  merge_mma1<15>(se, so); c.bad = __builtin_amdgcn_ballot_w64((c.orv[1] & 0x40004000u) != 0u); PIN_S(c.bad); dma_go<true, 3072>(sw1_0, c.voff, o.srdW); const float eb1_0_0 = aread1<17>(); SBAR();
  asm volatile("s_waitcnt vmcnt(10)" ::: ACLOB, "memory"); merge_mma1<16>(se, so); const float ea1_0_1 = aread1<18>(); const float eb1_0_1 = aread1<19>(); const float ea1_0_2 = aread1<24>(); SBAR();
  merge_mma1<17>(se, so); const unsigned sw1_1 = dma_prep<102400, 36864>(c.m0); const float eb1_0_2 = aread1<25>(); const float ea1_0_3 = aread1<26>(); SBAR();
  merge_mma1<18>(se, so); const float eb1_0_3 = aread1<27>(); const float fa1_0_0 = mul_asm(ea1_0_0, sc0); const float fb1_0_0 = mul_asm(eb1_0_0, sc0); const float fa1_0_1 = mul_asm(ea1_0_1, sc0); SBAR();
  merge_mma1<19>(se, so); dma_go<true, 0>(sw1_1, c.voff, o.srdW); const float fb1_0_1 = mul_asm(eb1_0_1, sc0); const float fa1_0_2 = mul_asm(ea1_0_2, sc0); const float fb1_0_2 = mul_asm(eb1_0_2, sc0); SBAR();
  merge_mma1<20>(se, so); const float fa1_0_3 = mul_asm(ea1_0_3, sc0); const float fb1_0_3 = mul_asm(eb1_0_3, sc0); cf[0][1][0] = cvt_asm(fa1_0_0, fb1_0_0); cf[0][1][1] = cvt_asm(fa1_0_1, fb1_0_1); SBAR();
  merge_mma1<21>(se, so); cf[0][1][2] = cvt_asm(fa1_0_2, fb1_0_2); cf[0][1][3] = cvt_asm(fa1_0_3, fb1_0_3); const float ea2_0_0 = aread1<32>(); const float eb2_0_0 = aread1<33>(); SBAR();
  merge_mma1<22>(se, so); const float ea2_0_1 = aread1<34>(); const float eb2_0_1 = aread1<35>(); const float ea2_0_2 = aread1<40>(); const float eb2_0_2 = aread1<41>(); SBAR();
  merge_mma1<23>(se, so); dma_go<true, 1024>(sw1_1, c.voff, o.srdW); const float ea2_0_3 = aread1<42>(); const float eb2_0_3 = aread1<43>(); const float fa2_0_0 = mul_asm(ea2_0_0, sc0); SBAR();
  asm volatile("s_waitcnt vmcnt(8)" ::: ACLOB, "memory"); merge_mma1<24>(se, so); const float fb2_0_0 = mul_asm(eb2_0_0, sc0); const float fa2_0_1 = mul_asm(ea2_0_1, sc0); const float fb2_0_1 = mul_asm(eb2_0_1, sc0); SBAR();
  merge_mma1<25>(se, so); const float fa2_0_2 = mul_asm(ea2_0_2, sc0); const float fb2_0_2 = mul_asm(eb2_0_2, sc0); const float fa2_0_3 = mul_asm(ea2_0_3, sc0); const float fb2_0_3 = mul_asm(eb2_0_3, sc0); SBAR();
  merge_mma1<26>(se, so); cf[0][2][0] = cvt_asm(fa2_0_0, fb2_0_0); cf[0][2][1] = cvt_asm(fa2_0_1, fb2_0_1); cf[0][2][2] = cvt_asm(fa2_0_2, fb2_0_2); cf[0][2][3] = cvt_asm(fa2_0_3, fb2_0_3); SBAR();
  merge_mma1<27>(se, so); dma_go<true, 2048>(sw1_1, c.voff, o.srdW); const float ea3_0_0 = aread1<48>(); const float eb3_0_0 = aread1<49>(); const float ea3_0_1 = aread1<50>(); SBAR();
  merge_mma1<28>(se, so); const float eb3_0_1 = aread1<51>(); const float ea3_0_2 = aread1<56>(); const float eb3_0_2 = aread1<57>(); const float ea3_0_3 = aread1<58>(); SBAR();
  merge_mma1<29>(se, so); const float eb3_0_3 = aread1<59>(); const float fa3_0_0 = mul_asm(ea3_0_0, sc0); const float fb3_0_0 = mul_asm(eb3_0_0, sc0); const float fa3_0_1 = mul_asm(ea3_0_1, sc0); SBAR();
  merge_mma1<30>(se, so); const float fb3_0_1 = mul_asm(eb3_0_1, sc0); const float fa3_0_2 = mul_asm(ea3_0_2, sc0); const float fb3_0_2 = mul_asm(eb3_0_2, sc0); const float fa3_0_3 = mul_asm(ea3_0_3, sc0); SBAR();
  merge_mma1<31>(se, so); dma_go<true, 3072>(sw1_1, c.voff, o.srdW); const float fb3_0_3 = mul_asm(eb3_0_3, sc0); cf[0][3][0] = cvt_asm(fa3_0_0, fb3_0_0); cf[0][3][1] = cvt_asm(fa3_0_1, fb3_0_1); SBAR();
  if (__builtin_expect(c.bad != 0ull, 0)) rescale<1, 2, 1>(c); asm volatile("s_nop 1" ::: "memory"); SBAR();
  MMA_V(c.L[1], c.ones, c.P[0][1]); const bf16x8 vf8 = ld128(c.vb + 36864); cf[0][3][2] = cvt_asm(fa3_0_2, fb3_0_2); cf[0][3][3] = cvt_asm(fa3_0_3, fb3_0_3); const float ea4_0_0 = aread1<64>(); SBAR();
  MMA_V(c.L[1], c.ones, c.P[1][1]); const bf16x8 vf9 = ld128(c.vb + 53248); const float eb4_0_0 = aread1<65>(); const float ea4_0_1 = aread1<66>(); const float eb4_0_1 = aread1<67>(); SBAR();
  asm volatile("s_waitcnt vmcnt(8)" ::: ACLOB, "memory"); mma_ab0<128>(ln[0], w.pq[0]); op_bases<0>(o, w, lane); const bf16x8 vf10 = ld128(c.vb + 37888); SBAR();
  mma_ab0<132>(ln[1], w.pq[0]); op_bases<1>(o, w, lane); const bf16x8 vf11 = ld128(c.vb + 54272); SBAR();
  mma_ab0<128>(ln[2], w.pq[1]); op_bases<2>(o, w, lane); const bf16x8 vf12 = ld128(c.vb + 38912); SBAR();
  mma_ab0<132>(ln[3], w.pq[1]); op_bases<3>(o, w, lane); const bf16x8 vf13 = ld128(c.vb + 55296); SBAR();
  mma_acc<4>(vf0, c.P[0][1]); const bf16x8 vf14 = ld128(c.vb + 39936); const float ea4_0_2 = aread1<72>(); const float eb4_0_2 = aread1<73>(); const float ea4_0_3 = aread1<74>(); SBAR();
  mma_acc<4>(vf1, c.P[1][1]); PIN_V(c.L[1]); float sc1 = w.g[1] * __builtin_amdgcn_rcpf(c.L[1][0]); PIN_V(sc1); const bf16x8 vf15 = ld128(c.vb + 56320); const unsigned sw2_0 = dma_prep<131072, 65536>(c.m0); SBAR();
  mma_acc<12>(vf2, c.P[0][1]); const bf16x8 vf16 = ld128(c.vb + 40960); const float eb4_0_3 = aread1<75>(); const float fa4_0_0 = mul_asm(ea4_0_0, sc0); const float fb4_0_0 = mul_asm(eb4_0_0, sc0); SBAR();
  mma_acc<12>(vf3, c.P[1][1]); const bf16x8 vf17 = ld128(c.vb + 57344); dma_go<true, 0>(sw2_0, c.voff, o.srdW); const float fa4_0_1 = mul_asm(ea4_0_1, sc0); const float fb4_0_1 = mul_asm(eb4_0_1, sc0); SBAR();
  mma_acc<20>(vf4, c.P[0][1]); T_LIN_A(); const bf16x8 vf18 = ld128(c.vb + 41984); SBAR();
  mma_acc<20>(vf5, c.P[1][1]); const bf16x8 vf19 = ld128(c.vb + 58368); const float fa4_0_2 = mul_asm(ea4_0_2, sc0); const float fb4_0_2 = mul_asm(eb4_0_2, sc0); const float fa4_0_3 = mul_asm(ea4_0_3, sc0); SBAR();
  mma_acc<28>(vf6, c.P[0][1]); const bf16x8 vf20 = ld128(c.vb + 43008); const float fb4_0_3 = mul_asm(eb4_0_3, sc0); cf[0][4][0] = cvt_asm(fa4_0_0, fb4_0_0); cf[0][4][1] = cvt_asm(fa4_0_1, fb4_0_1); SBAR();
  mma_acc<28>(vf7, c.P[1][1]); const bf16x8 vf21 = ld128(c.vb + 59392); dma_go<true, 1024>(sw2_0, c.voff, o.srdW); cf[0][4][2] = cvt_asm(fa4_0_2, fb4_0_2); cf[0][4][3] = cvt_asm(fa4_0_3, fb4_0_3); SBAR();
  LGKM(2); mma_acc<36>(vf8, c.P[0][1]); const bf16x8 vf22 = ld128(c.vb + 44032); const float ea0_1_0 = aread1<4>(); const float eb0_1_0 = aread1<5>(); const float ea0_1_1 = aread1<6>(); SBAR();
  mma_acc<36>(vf9, c.P[1][1]); const bf16x8 vf23 = ld128(c.vb + 60416); const float eb0_1_1 = aread1<7>(); const float ea0_1_2 = aread1<12>(); const float eb0_1_2 = aread1<13>(); SBAR();
  mma_acc<44>(vf10, c.P[0][1]); const bf16x8 vf24 = ld128(c.vb + 45056); const float ea0_1_3 = aread1<14>(); const float eb0_1_3 = aread1<15>(); const float fa0_1_0 = mul_asm(ea0_1_0, sc1); SBAR();
  mma_acc<44>(vf11, c.P[1][1]); const bf16x8 vf25 = ld128(c.vb + 61440); dma_go<true, 2048>(sw2_0, c.voff, o.srdW); const float fb0_1_0 = mul_asm(eb0_1_0, sc1); const float fa0_1_1 = mul_asm(ea0_1_1, sc1); SBAR();
  mma_acc<52>(vf12, c.P[0][1]); const bf16x8 vf26 = ld128(c.vb + 46080); const float fb0_1_1 = mul_asm(eb0_1_1, sc1); const float fa0_1_2 = mul_asm(ea0_1_2, sc1); const float fb0_1_2 = mul_asm(eb0_1_2, sc1); SBAR();
  mma_acc<52>(vf13, c.P[1][1]); const bf16x8 vf27 = ld128(c.vb + 62464); const float fa0_1_3 = mul_asm(ea0_1_3, sc1); const float fb0_1_3 = mul_asm(eb0_1_3, sc1); cf[1][0][0] = cvt_asm(fa0_1_0, fb0_1_0); SBAR();
  mma_acc<60>(vf14, c.P[0][1]); const bf16x8 vf28 = ld128(c.vb + 47104); cf[1][0][1] = cvt_asm(fa0_1_1, fb0_1_1); cf[1][0][2] = cvt_asm(fa0_1_2, fb0_1_2); cf[1][0][3] = cvt_asm(fa0_1_3, fb0_1_3); SBAR();
  mma_acc<60>(vf15, c.P[1][1]); const bf16x8 vf29 = ld128(c.vb + 63488); dma_go<true, 3072>(sw2_0, c.voff, o.srdW); T_LIN_B(); SBAR();
  mma_acc<68>(vf16, c.P[0][1]); const bf16x8 vf30 = ld128(c.vb + 48128); const float ea1_1_0 = aread1<20>(); const float eb1_1_0 = aread1<21>(); const float ea1_1_1 = aread1<22>(); SBAR();
  mma_acc<68>(vf17, c.P[1][1]); const bf16x8 vf31 = ld128(c.vb + 64512); const unsigned sw2_1 = dma_prep<135168, 69632>(c.m0); const float eb1_1_1 = aread1<23>(); SBAR();
  mma_acc<76>(vf18, c.P[0][1]); const float ea1_1_2 = aread1<28>(); const float eb1_1_2 = aread1<29>(); const float ea1_1_3 = aread1<30>(); const float eb1_1_3 = aread1<31>(); SBAR();
  mma_acc<76>(vf19, c.P[1][1]); dma_go<true, 0>(sw2_1, c.voff, o.srdW); const float fa1_1_0 = mul_asm(ea1_1_0, sc1); const float fb1_1_0 = mul_asm(eb1_1_0, sc1); const float fa1_1_1 = mul_asm(ea1_1_1, sc1); SBAR();
  LGKM(0); mma_acc<84>(vf20, c.P[0][1]); const s16x4 u0_0_0 = ldtr(o.wb[1][0] + 0); const float fb1_1_1 = mul_asm(eb1_1_1, sc1); const float fa1_1_2 = mul_asm(ea1_1_2, sc1); const float fb1_1_2 = mul_asm(eb1_1_2, sc1); SBAR();
  mma_acc<84>(vf21, c.P[1][1]); const s16x4 u0_0_1 = ldtr(o.wb[1][0] + 2048); const float fa1_1_3 = mul_asm(ea1_1_3, sc1); const float fb1_1_3 = mul_asm(eb1_1_3, sc1); cf[1][1][0] = cvt_asm(fa1_1_0, fb1_1_0); SBAR();
  mma_acc<92>(vf22, c.P[0][1]); const s16x4 u0_1_0 = ldtr(o.wb[1][1] + 0); cf[1][1][1] = cvt_asm(fa1_1_1, fb1_1_1); cf[1][1][2] = cvt_asm(fa1_1_2, fb1_1_2); cf[1][1][3] = cvt_asm(fa1_1_3, fb1_1_3); SBAR();
  mma_acc<92>(vf23, c.P[1][1]); const s16x4 u0_1_1 = ldtr(o.wb[1][1] + 2048); dma_go<true, 1024>(sw2_1, c.voff, o.srdW); const float ea2_1_0 = aread1<36>(); const float eb2_1_0 = aread1<37>(); SBAR();
  mma_acc<100>(vf24, c.P[0][1]); const s16x4 u0_2_0 = ldtr(o.wb[1][2] + 0); const float ea2_1_1 = aread1<38>(); const float eb2_1_1 = aread1<39>(); const float ea2_1_2 = aread1<44>(); SBAR();
  mma_acc<100>(vf25, c.P[1][1]); const s16x4 u0_2_1 = ldtr(o.wb[1][2] + 2048); const float eb2_1_2 = aread1<45>(); const float ea2_1_3 = aread1<46>(); const float eb2_1_3 = aread1<47>(); SBAR();
  mma_acc<108>(vf26, c.P[0][1]); const s16x4 u0_3_0 = ldtr(o.wb[1][3] + 0); const float fa2_1_0 = mul_asm(ea2_1_0, sc1); const float fb2_1_0 = mul_asm(eb2_1_0, sc1); const float fa2_1_1 = mul_asm(ea2_1_1, sc1); SBAR();
  mma_acc<108>(vf27, c.P[1][1]); const s16x4 u0_3_1 = ldtr(o.wb[1][3] + 2048); dma_go<true, 2048>(sw2_1, c.voff, o.srdW); const float fb2_1_1 = mul_asm(eb2_1_1, sc1); const float fa2_1_2 = mul_asm(ea2_1_2, sc1); SBAR();
  mma_acc<116>(vf28, c.P[0][1]); const s16x4 u1_0_0 = ldtr(o.wb[1][0] + 4096); const float fb2_1_2 = mul_asm(eb2_1_2, sc1); const float fa2_1_3 = mul_asm(ea2_1_3, sc1); const float fb2_1_3 = mul_asm(eb2_1_3, sc1); SBAR();
  mma_acc<116>(vf29, c.P[1][1]); const s16x4 u1_0_1 = ldtr(o.wb[1][0] + 6144); cf[1][2][0] = cvt_asm(fa2_1_0, fb2_1_0); cf[1][2][1] = cvt_asm(fa2_1_1, fb2_1_1); cf[1][2][2] = cvt_asm(fa2_1_2, fb2_1_2); SBAR();
  mma_acc<124>(vf30, c.P[0][1]); const s16x4 u1_1_0 = ldtr(o.wb[1][1] + 4096); cf[1][2][3] = cvt_asm(fa2_1_3, fb2_1_3); const float ea3_1_0 = aread1<52>(); const float eb3_1_0 = aread1<53>(); SBAR();
  mma_acc<124>(vf31, c.P[1][1]); const s16x4 u1_1_1 = ldtr(o.wb[1][1] + 6144); dma_go<true, 3072>(sw2_1, c.voff, o.srdW); const float ea3_1_1 = aread1<54>(); const float eb3_1_1 = aread1<55>(); SBAR();
  LGKM(5); MMA_V0(acc[0][0][0], cat(u0_0_0, u0_0_1), cf[0][8]); const s16x4 u1_2_0 = ldtr(o.wb[1][2] + 4096); const float ea3_1_2 = aread1<60>(); const float eb3_1_2 = aread1<61>(); const float ea3_1_3 = aread1<62>(); SBAR();
  MMA_V0(acc[0][0][1], cat(u0_0_0, u0_0_1), cf[1][8]); const s16x4 u1_2_1 = ldtr(o.wb[1][2] + 6144); const s16x4 u1_3_0 = ldtr(o.wb[1][3] + 4096); const float eb3_1_3 = aread1<63>(); const float fa3_1_0 = mul_asm(ea3_1_0, sc1); SBAR();
  MMA_V0(acc[0][1][0], cat(u0_1_0, u0_1_1), cf[0][8]); const s16x4 u1_3_1 = ldtr(o.wb[1][3] + 6144); const unsigned sw3_0 = dma_prep<0, 98304>(c.m0); const float fb3_1_0 = mul_asm(eb3_1_0, sc1); SBAR();
  MMA_V0(acc[0][1][1], cat(u0_1_0, u0_1_1), cf[1][8]); const s16x4 u2_0_0 = ldtr(o.wb[1][0] + 8192); dma_go<true, 0>(sw3_0, c.voff, o.srdW); const float fa3_1_1 = mul_asm(ea3_1_1, sc1); const float fb3_1_1 = mul_asm(eb3_1_1, sc1); SBAR();
  MMA_V0(acc[0][2][0], cat(u0_2_0, u0_2_1), cf[0][8]); const s16x4 u2_0_1 = ldtr(o.wb[1][0] + 10240); const float fa3_1_2 = mul_asm(ea3_1_2, sc1); const float fb3_1_2 = mul_asm(eb3_1_2, sc1); const float fa3_1_3 = mul_asm(ea3_1_3, sc1); SBAR();
  MMA_V0(acc[0][2][1], cat(u0_2_0, u0_2_1), cf[1][8]); const s16x4 u2_1_0 = ldtr(o.wb[1][1] + 8192); const s16x4 u2_1_1 = ldtr(o.wb[1][1] + 10240); const float fb3_1_3 = mul_asm(eb3_1_3, sc1); cf[1][3][0] = cvt_asm(fa3_1_0, fb3_1_0); SBAR();
  LGKM(7); MMA_V0(acc[0][3][0], cat(u0_3_0, u0_3_1), cf[0][8]); const s16x4 u2_2_0 = ldtr(o.wb[1][2] + 8192); cf[1][3][1] = cvt_asm(fa3_1_1, fb3_1_1); cf[1][3][2] = cvt_asm(fa3_1_2, fb3_1_2); cf[1][3][3] = cvt_asm(fa3_1_3, fb3_1_3); SBAR();
  MMA_V0(acc[0][3][1], cat(u0_3_0, u0_3_1), cf[1][8]); const s16x4 u2_2_1 = ldtr(o.wb[1][2] + 10240); dma_go<true, 1024>(sw3_0, c.voff, o.srdW); const float ea4_1_0 = aread1<68>(); const float eb4_1_0 = aread1<69>(); SBAR();
  MMA_V(acc[0][0][0], cat(u1_0_0, u1_0_1), cf[0][0]); const s16x4 u2_3_0 = ldtr(o.wb[1][3] + 8192); const float ea4_1_1 = aread1<70>(); const float eb4_1_1 = aread1<71>(); const float ea4_1_2 = aread1<76>(); SBAR();
  MMA_V(acc[0][0][1], cat(u1_0_0, u1_0_1), cf[1][0]); const s16x4 u2_3_1 = ldtr(o.wb[1][3] + 10240); const s16x4 u3_0_0 = ldtr(o.wb[1][0] + 12288); const float eb4_1_2 = aread1<77>(); const float ea4_1_3 = aread1<78>(); SBAR();
  MMA_V(acc[0][1][0], cat(u1_1_0, u1_1_1), cf[0][0]); const s16x4 u3_0_1 = ldtr(o.wb[1][0] + 14336); const float eb4_1_3 = aread1<79>(); const float fa4_1_0 = mul_asm(ea4_1_0, sc1); const float fb4_1_0 = mul_asm(eb4_1_0, sc1); SBAR();
  MMA_V(acc[0][1][1], cat(u1_1_0, u1_1_1), cf[1][0]); const s16x4 u3_1_0 = ldtr(o.wb[1][1] + 12288); dma_go<true, 2048>(sw3_0, c.voff, o.srdW); const float fa4_1_1 = mul_asm(ea4_1_1, sc1); const float fb4_1_1 = mul_asm(eb4_1_1, sc1); SBAR();
  LGKM(6); MMA_V(acc[0][2][0], cat(u1_2_0, u1_2_1), cf[0][0]); const s16x4 u3_1_1 = ldtr(o.wb[1][1] + 14336); const float fa4_1_2 = mul_asm(ea4_1_2, sc1); const float fb4_1_2 = mul_asm(eb4_1_2, sc1); const float fa4_1_3 = mul_asm(ea4_1_3, sc1); SBAR();
  MMA_V(acc[0][2][1], cat(u1_2_0, u1_2_1), cf[1][0]); const s16x4 u3_2_0 = ldtr(o.wb[1][2] + 12288); const s16x4 u3_2_1 = ldtr(o.wb[1][2] + 14336); const float fb4_1_3 = mul_asm(eb4_1_3, sc1); cf[1][4][0] = cvt_asm(fa4_1_0, fb4_1_0); SBAR();
  MMA_V(acc[0][3][0], cat(u1_3_0, u1_3_1), cf[0][0]); const s16x4 u3_3_0 = ldtr(o.wb[1][3] + 12288); cf[1][4][1] = cvt_asm(fa4_1_1, fb4_1_1); cf[1][4][2] = cvt_asm(fa4_1_2, fb4_1_2); cf[1][4][3] = cvt_asm(fa4_1_3, fb4_1_3); SBAR();
  MMA_V(acc[0][3][1], cat(u1_3_0, u1_3_1), cf[1][0]); const s16x4 u3_3_1 = ldtr(o.wb[1][3] + 14336); dma_go<true, 3072>(sw3_0, c.voff, o.srdW); const float ea5_0_0 = aread1<80>(); const float eb5_0_0 = aread1<81>(); SBAR();
  MMA_V(acc[0][0][0], cat(u2_0_0, u2_0_1), cf[0][1]); const s16x4 u4_0_0 = ldtr(o.wb[1][0] + 16384); const float ea5_0_1 = aread1<82>(); const float eb5_0_1 = aread1<83>(); const float ea5_0_2 = aread1<88>(); SBAR();
  MMA_V(acc[0][0][1], cat(u2_0_0, u2_0_1), cf[1][1]); const s16x4 u4_0_1 = ldtr(o.wb[1][0] + 18432); const s16x4 u4_1_0 = ldtr(o.wb[1][1] + 16384); const float eb5_0_2 = aread1<89>(); const float ea5_0_3 = aread1<90>(); SBAR();
  MMA_V(acc[0][1][0], cat(u2_1_0, u2_1_1), cf[0][1]); const s16x4 u4_1_1 = ldtr(o.wb[1][1] + 18432); const unsigned sw3_1 = dma_prep<4096, 102400>(c.m0); const float eb5_0_3 = aread1<91>(); SBAR();
  MMA_V(acc[0][1][1], cat(u2_1_0, u2_1_1), cf[1][1]); const s16x4 u4_2_0 = ldtr(o.wb[1][2] + 16384); dma_go<true, 0>(sw3_1, c.voff, o.srdW); const float fa5_0_0 = mul_asm(ea5_0_0, sc0); const float fb5_0_0 = mul_asm(eb5_0_0, sc0); SBAR();
  LGKM(6); MMA_V(acc[0][2][0], cat(u2_2_0, u2_2_1), cf[0][1]); const s16x4 u4_2_1 = ldtr(o.wb[1][2] + 18432); const float fa5_0_1 = mul_asm(ea5_0_1, sc0); const float fb5_0_1 = mul_asm(eb5_0_1, sc0); const float fa5_0_2 = mul_asm(ea5_0_2, sc0); SBAR();
  MMA_V(acc[0][2][1], cat(u2_2_0, u2_2_1), cf[1][1]); const s16x4 u4_3_0 = ldtr(o.wb[1][3] + 16384); const s16x4 u4_3_1 = ldtr(o.wb[1][3] + 18432); const float fb5_0_2 = mul_asm(eb5_0_2, sc0); const float fa5_0_3 = mul_asm(ea5_0_3, sc0); SBAR();
  MMA_V(acc[0][3][0], cat(u2_3_0, u2_3_1), cf[0][1]); const s16x4 u5_0_0 = ldtr(o.wb[1][0] + 20480); const float fb5_0_3 = mul_asm(eb5_0_3, sc0); cf[0][5][0] = cvt_asm(fa5_0_0, fb5_0_0); cf[0][5][1] = cvt_asm(fa5_0_1, fb5_0_1); SBAR();
  MMA_V(acc[0][3][1], cat(u2_3_0, u2_3_1), cf[1][1]); const s16x4 u5_0_1 = ldtr(o.wb[1][0] + 22528); dma_go<true, 1024>(sw3_1, c.voff, o.srdW); cf[0][5][2] = cvt_asm(fa5_0_2, fb5_0_2); cf[0][5][3] = cvt_asm(fa5_0_3, fb5_0_3); SBAR();
  MMA_V(acc[0][0][0], cat(u3_0_0, u3_0_1), cf[0][2]); const s16x4 u5_1_0 = ldtr(o.wb[1][1] + 20480); const float ea5_1_0 = aread1<84>(); const float eb5_1_0 = aread1<85>(); const float ea5_1_1 = aread1<86>(); SBAR();
  MMA_V(acc[0][0][1], cat(u3_0_0, u3_0_1), cf[1][2]); const s16x4 u5_1_1 = ldtr(o.wb[1][1] + 22528); const float eb5_1_1 = aread1<87>(); const float ea5_1_2 = aread1<92>(); const float eb5_1_2 = aread1<93>(); SBAR();
  MMA_V(acc[0][1][0], cat(u3_1_0, u3_1_1), cf[0][2]); const s16x4 u5_2_0 = ldtr(o.wb[1][2] + 20480); const float ea5_1_3 = aread1<94>(); const float eb5_1_3 = aread1<95>(); const float fa5_1_0 = mul_asm(ea5_1_0, sc1); SBAR();
  MMA_V(acc[0][1][1], cat(u3_1_0, u3_1_1), cf[1][2]); const s16x4 u5_2_1 = ldtr(o.wb[1][2] + 22528); dma_go<true, 2048>(sw3_1, c.voff, o.srdW); const float fb5_1_0 = mul_asm(eb5_1_0, sc1); const float fa5_1_1 = mul_asm(ea5_1_1, sc1); SBAR();
  MMA_V(acc[0][2][0], cat(u3_2_0, u3_2_1), cf[0][2]); const s16x4 u5_3_0 = ldtr(o.wb[1][3] + 20480); const float fb5_1_1 = mul_asm(eb5_1_1, sc1); const float fa5_1_2 = mul_asm(ea5_1_2, sc1); const float fb5_1_2 = mul_asm(eb5_1_2, sc1); SBAR();
  MMA_V(acc[0][2][1], cat(u3_2_0, u3_2_1), cf[1][2]); const s16x4 u5_3_1 = ldtr(o.wb[1][3] + 22528); const float fa5_1_3 = mul_asm(ea5_1_3, sc1); const float fb5_1_3 = mul_asm(eb5_1_3, sc1); cf[1][5][0] = cvt_asm(fa5_1_0, fb5_1_0); SBAR();
  LGKM(5); MMA_V(acc[0][3][0], cat(u3_3_0, u3_3_1), cf[0][2]); const s16x4 u6_0_0 = ldtr(o.wb[1][0] + 24576); cf[1][5][1] = cvt_asm(fa5_1_1, fb5_1_1); cf[1][5][2] = cvt_asm(fa5_1_2, fb5_1_2); cf[1][5][3] = cvt_asm(fa5_1_3, fb5_1_3); SBAR();
  MMA_V(acc[0][3][1], cat(u3_3_0, u3_3_1), cf[1][2]); const s16x4 u6_0_1 = ldtr(o.wb[1][0] + 26624); dma_go<true, 3072>(sw3_1, c.voff, o.srdW); const float ea6_0_0 = aread1<96>(); const float eb6_0_0 = aread1<97>(); SBAR();
  MMA_V(acc[0][0][0], cat(u4_0_0, u4_0_1), cf[0][3]); const s16x4 u6_1_0 = ldtr(o.wb[1][1] + 24576); const float ea6_0_1 = aread1<98>(); const float eb6_0_1 = aread1<99>(); const float ea6_0_2 = aread1<104>(); SBAR();
  MMA_V(acc[0][0][1], cat(u4_0_0, u4_0_1), cf[1][3]); const s16x4 u6_1_1 = ldtr(o.wb[1][1] + 26624); const float eb6_0_2 = aread1<105>(); const float ea6_0_3 = aread1<106>(); const float eb6_0_3 = aread1<107>(); SBAR();
  MMA_V(acc[0][1][0], cat(u4_1_0, u4_1_1), cf[0][3]); const s16x4 u6_2_0 = ldtr(o.wb[1][2] + 24576); const float fa6_0_0 = mul_asm(ea6_0_0, sc0); const float fb6_0_0 = mul_asm(eb6_0_0, sc0); const float fa6_0_1 = mul_asm(ea6_0_1, sc0); SBAR();
  MMA_V(acc[0][1][1], cat(u4_1_0, u4_1_1), cf[1][3]); const s16x4 u6_2_1 = ldtr(o.wb[1][2] + 26624); const float fb6_0_1 = mul_asm(eb6_0_1, sc0); const float fa6_0_2 = mul_asm(ea6_0_2, sc0); const float fb6_0_2 = mul_asm(eb6_0_2, sc0); SBAR();
  MMA_V(acc[0][2][0], cat(u4_2_0, u4_2_1), cf[0][3]); const s16x4 u6_3_0 = ldtr(o.wb[1][3] + 24576); const float fa6_0_3 = mul_asm(ea6_0_3, sc0); const float fb6_0_3 = mul_asm(eb6_0_3, sc0); cf[0][6][0] = cvt_asm(fa6_0_0, fb6_0_0); SBAR();
  MMA_V(acc[0][2][1], cat(u4_2_0, u4_2_1), cf[1][3]); const s16x4 u6_3_1 = ldtr(o.wb[1][3] + 26624); cf[0][6][1] = cvt_asm(fa6_0_1, fb6_0_1); cf[0][6][2] = cvt_asm(fa6_0_2, fb6_0_2); cf[0][6][3] = cvt_asm(fa6_0_3, fb6_0_3); SBAR();
  MMA_V(acc[0][3][0], cat(u4_3_0, u4_3_1), cf[0][3]); const s16x4 u7_0_0 = ldtr(o.wb[1][0] + 28672); const float ea6_1_0 = aread1<100>(); const float eb6_1_0 = aread1<101>(); const float ea6_1_1 = aread1<102>(); SBAR();
  MMA_V(acc[0][3][1], cat(u4_3_0, u4_3_1), cf[1][3]); const s16x4 u7_0_1 = ldtr(o.wb[1][0] + 30720); const float eb6_1_1 = aread1<103>(); const float ea6_1_2 = aread1<108>(); const float eb6_1_2 = aread1<109>(); SBAR();
  MMA_V(acc[0][0][0], cat(u5_0_0, u5_0_1), cf[0][4]); const s16x4 u7_1_0 = ldtr(o.wb[1][1] + 28672); const float ea6_1_3 = aread1<110>(); const float eb6_1_3 = aread1<111>(); const float fa6_1_0 = mul_asm(ea6_1_0, sc1); SBAR();
  MMA_V(acc[0][0][1], cat(u5_0_0, u5_0_1), cf[1][4]); const s16x4 u7_1_1 = ldtr(o.wb[1][1] + 30720); const float fb6_1_0 = mul_asm(eb6_1_0, sc1); const float fa6_1_1 = mul_asm(ea6_1_1, sc1); const float fb6_1_1 = mul_asm(eb6_1_1, sc1); SBAR();
  LGKM(5); MMA_V(acc[0][1][0], cat(u5_1_0, u5_1_1), cf[0][4]); const s16x4 u7_2_0 = ldtr(o.wb[1][2] + 28672); const float fa6_1_2 = mul_asm(ea6_1_2, sc1); const float fb6_1_2 = mul_asm(eb6_1_2, sc1); const float fa6_1_3 = mul_asm(ea6_1_3, sc1); SBAR();
  MMA_V(acc[0][1][1], cat(u5_1_0, u5_1_1), cf[1][4]); const s16x4 u7_2_1 = ldtr(o.wb[1][2] + 30720); const float fb6_1_3 = mul_asm(eb6_1_3, sc1); cf[1][6][0] = cvt_asm(fa6_1_0, fb6_1_0); cf[1][6][1] = cvt_asm(fa6_1_1, fb6_1_1); SBAR();
  MMA_V(acc[0][2][0], cat(u5_2_0, u5_2_1), cf[0][4]); const s16x4 u7_3_0 = ldtr(o.wb[1][3] + 28672); cf[1][6][2] = cvt_asm(fa6_1_2, fb6_1_2); cf[1][6][3] = cvt_asm(fa6_1_3, fb6_1_3); const float ea7_0_0 = aread1<112>(); SBAR();
  MMA_V(acc[0][2][1], cat(u5_2_0, u5_2_1), cf[1][4]); const s16x4 u7_3_1 = ldtr(o.wb[1][3] + 30720); const float eb7_0_0 = aread1<113>(); const float ea7_0_1 = aread1<114>(); const float eb7_0_1 = aread1<115>(); SBAR();
  MMA_V(acc[0][3][0], cat(u5_3_0, u5_3_1), cf[0][4]); const float ea7_0_2 = aread1<120>(); const float eb7_0_2 = aread1<121>(); const float ea7_0_3 = aread1<122>(); const float eb7_0_3 = aread1<123>(); SBAR();
  MMA_V(acc[0][3][1], cat(u5_3_0, u5_3_1), cf[1][4]); const float fa7_0_0 = mul_asm(ea7_0_0, sc0); const float fb7_0_0 = mul_asm(eb7_0_0, sc0); const float fa7_0_1 = mul_asm(ea7_0_1, sc0); const float fb7_0_1 = mul_asm(eb7_0_1, sc0); SBAR();
  MMA_V(acc[0][0][0], cat(u6_0_0, u6_0_1), cf[0][5]); const float fa7_0_2 = mul_asm(ea7_0_2, sc0); const float fb7_0_2 = mul_asm(eb7_0_2, sc0); const float fa7_0_3 = mul_asm(ea7_0_3, sc0); const float fb7_0_3 = mul_asm(eb7_0_3, sc0); SBAR();
  MMA_V(acc[0][0][1], cat(u6_0_0, u6_0_1), cf[1][5]); cf[0][7][0] = cvt_asm(fa7_0_0, fb7_0_0); cf[0][7][1] = cvt_asm(fa7_0_1, fb7_0_1); cf[0][7][2] = cvt_asm(fa7_0_2, fb7_0_2); cf[0][7][3] = cvt_asm(fa7_0_3, fb7_0_3); SBAR();
  MMA_V(acc[0][1][0], cat(u6_1_0, u6_1_1), cf[0][5]); LGKM(0); asm volatile("s_waitcnt vmcnt(16)\n\ts_barrier" ::: ACLOB, "memory"); SBAR();
  MMA_V(acc[0][1][1], cat(u6_1_0, u6_1_1), cf[1][5]); const s16x4 u8_0_0 = ldtr(o.wb[1][0] + 32768); const unsigned sw4_0 = dma_prep<32768, 131072>(c.m0); dma_go<true, 0>(sw4_0, c.voff, o.srdW); SBAR();
  MMA_V(acc[0][2][0], cat(u6_2_0, u6_2_1), cf[0][5]); const s16x4 u8_0_1 = ldtr(o.wb[1][0] + 34816); const float ea7_1_0 = aread1<116>(); const float eb7_1_0 = aread1<117>(); const float ea7_1_1 = aread1<118>(); SBAR();
  MMA_V(acc[0][2][1], cat(u6_2_0, u6_2_1), cf[1][5]); const s16x4 u8_1_0 = ldtr(o.wb[1][1] + 32768); const s16x4 u8_1_1 = ldtr(o.wb[1][1] + 34816); const float eb7_1_1 = aread1<119>(); const float ea7_1_2 = aread1<124>(); SBAR();
  MMA_V(acc[0][3][0], cat(u6_3_0, u6_3_1), cf[0][5]); const s16x4 u8_2_0 = ldtr(o.wb[1][2] + 32768); const float eb7_1_2 = aread1<125>(); const float ea7_1_3 = aread1<126>(); const float eb7_1_3 = aread1<127>(); SBAR();
  MMA_V(acc[0][3][1], cat(u6_3_0, u6_3_1), cf[1][5]); const s16x4 u8_2_1 = ldtr(o.wb[1][2] + 34816); dma_go<true, 1024>(sw4_0, c.voff, o.srdW); const float fa7_1_0 = mul_asm(ea7_1_0, sc1); const float fb7_1_0 = mul_asm(eb7_1_0, sc1); SBAR();
  MMA_V(acc[0][0][0], cat(u7_0_0, u7_0_1), cf[0][6]); const s16x4 u8_3_0 = ldtr(o.wb[1][3] + 32768); const float fa7_1_1 = mul_asm(ea7_1_1, sc1); const float fb7_1_1 = mul_asm(eb7_1_1, sc1); const float fa7_1_2 = mul_asm(ea7_1_2, sc1); SBAR();
  MMA_V(acc[0][0][1], cat(u7_0_0, u7_0_1), cf[1][6]); const s16x4 u8_3_1 = ldtr(o.wb[1][3] + 34816); const s16x4 u9_0_0 = ldtr(o.wb[1][0] + 36864); const float fb7_1_2 = mul_asm(eb7_1_2, sc1); const float fa7_1_3 = mul_asm(ea7_1_3, sc1); SBAR();
  MMA_V(acc[0][1][0], cat(u7_1_0, u7_1_1), cf[0][6]); const s16x4 u9_0_1 = ldtr(o.wb[1][0] + 38912); const float fb7_1_3 = mul_asm(eb7_1_3, sc1); cf[1][7][0] = cvt_asm(fa7_1_0, fb7_1_0); cf[1][7][1] = cvt_asm(fa7_1_1, fb7_1_1); SBAR();
  MMA_V(acc[0][1][1], cat(u7_1_0, u7_1_1), cf[1][6]); const s16x4 u9_1_0 = ldtr(o.wb[1][1] + 36864); dma_go<true, 2048>(sw4_0, c.voff, o.srdW); cf[1][7][2] = cvt_asm(fa7_1_2, fb7_1_2); cf[1][7][3] = cvt_asm(fa7_1_3, fb7_1_3); SBAR();
  MMA_V(acc[0][2][0], cat(u7_2_0, u7_2_1), cf[0][6]); const s16x4 u9_1_1 = ldtr(o.wb[1][1] + 38912); SBAR();
  MMA_V(acc[0][2][1], cat(u7_2_0, u7_2_1), cf[1][6]); const s16x4 u9_2_0 = ldtr(o.wb[1][2] + 36864); const s16x4 u9_2_1 = ldtr(o.wb[1][2] + 38912); SBAR();
  MMA_V(acc[0][3][0], cat(u7_3_0, u7_3_1), cf[0][6]); const s16x4 u9_3_0 = ldtr(o.wb[1][3] + 36864); SBAR();
  MMA_V(acc[0][3][1], cat(u7_3_0, u7_3_1), cf[1][6]); const s16x4 u9_3_1 = ldtr(o.wb[1][3] + 38912); dma_go<true, 3072>(sw4_0, c.voff, o.srdW); SBAR();
  LGKM(6); MMA_V(acc[0][0][0], cat(u8_0_0, u8_0_1), cf[0][7]); const s16x4 u10_0_0 = ldtr(o.wb[1][0] + 40960); SBAR();
  MMA_V(acc[0][0][1], cat(u8_0_0, u8_0_1), cf[1][7]); const s16x4 u10_0_1 = ldtr(o.wb[1][0] + 43008); const s16x4 u10_1_0 = ldtr(o.wb[1][1] + 40960); SBAR();
  MMA_V(acc[0][1][0], cat(u8_1_0, u8_1_1), cf[0][7]); const s16x4 u10_1_1 = ldtr(o.wb[1][1] + 43008); const unsigned sw4_1 = dma_prep<36864, 135168>(c.m0); SBAR();
  MMA_V(acc[0][1][1], cat(u8_1_0, u8_1_1), cf[1][7]); const s16x4 u10_2_0 = ldtr(o.wb[1][2] + 40960); dma_go<true, 0>(sw4_1, c.voff, o.srdW); SBAR();
  MMA_V(acc[0][2][0], cat(u8_2_0, u8_2_1), cf[0][7]); const s16x4 u10_2_1 = ldtr(o.wb[1][2] + 43008); SBAR();
  MMA_V(acc[0][2][1], cat(u8_2_0, u8_2_1), cf[1][7]); const s16x4 u10_3_0 = ldtr(o.wb[1][3] + 40960); const s16x4 u10_3_1 = ldtr(o.wb[1][3] + 43008); SBAR();
  MMA_V(acc[0][3][0], cat(u8_3_0, u8_3_1), cf[0][7]); const s16x4 u11_0_0 = ldtr(o.wb[1][0] + 45056); SBAR();
  MMA_V(acc[0][3][1], cat(u8_3_0, u8_3_1), cf[1][7]); const s16x4 u11_0_1 = ldtr(o.wb[1][0] + 47104); dma_go<true, 1024>(sw4_1, c.voff, o.srdW); SBAR();
  MMA_V0(acc[1][0][0], cat(u9_0_0, u9_0_1), cf[0][8]); const s16x4 u11_1_0 = ldtr(o.wb[1][1] + 45056); SBAR();
  MMA_V0(acc[1][0][1], cat(u9_0_0, u9_0_1), cf[1][8]); const s16x4 u11_1_1 = ldtr(o.wb[1][1] + 47104); SBAR();
  LGKM(6); MMA_V0(acc[1][1][0], cat(u9_1_0, u9_1_1), cf[0][8]); const s16x4 u11_2_0 = ldtr(o.wb[1][2] + 45056); SBAR();
  MMA_V0(acc[1][1][1], cat(u9_1_0, u9_1_1), cf[1][8]); const s16x4 u11_2_1 = ldtr(o.wb[1][2] + 47104); dma_go<true, 2048>(sw4_1, c.voff, o.srdW); SBAR();
  MMA_V0(acc[1][2][0], cat(u9_2_0, u9_2_1), cf[0][8]); const s16x4 u11_3_0 = ldtr(o.wb[1][3] + 45056); SBAR();
  MMA_V0(acc[1][2][1], cat(u9_2_0, u9_2_1), cf[1][8]); const s16x4 u11_3_1 = ldtr(o.wb[1][3] + 47104); SBAR();
  MMA_V0(acc[1][3][0], cat(u9_3_0, u9_3_1), cf[0][8]); const s16x4 u12_0_0 = ldtr(o.wb[1][0] + 49152); const unsigned x0_0_0 = cvt_asm(acc[0][0][0][0], acc[0][0][0][1]); SBAR();
  MMA_V0(acc[1][3][1], cat(u9_3_0, u9_3_1), cf[1][8]); const s16x4 u12_0_1 = ldtr(o.wb[1][0] + 51200); dma_go<true, 3072>(sw4_1, c.voff, o.srdW); SBAR();
  MMA_V(acc[1][0][0], cat(u10_0_0, u10_0_1), cf[0][0]); const s16x4 u12_1_0 = ldtr(o.wb[1][1] + 49152); const unsigned x0_0_1 = cvt_asm(acc[0][0][0][2], acc[0][0][0][3]); SBAR();
  MMA_V(acc[1][0][1], cat(u10_0_0, u10_0_1), cf[1][0]); const s16x4 u12_1_1 = ldtr(o.wb[1][1] + 51200); const unsigned x0_0_2 = cvt_asm(acc[0][1][0][0], acc[0][1][0][1]); SBAR();
  MMA_V(acc[1][1][0], cat(u10_1_0, u10_1_1), cf[0][0]); const s16x4 u12_2_0 = ldtr(o.wb[1][2] + 49152); const unsigned sw5_0 = dma_prep<65536, 163840>(c.m0); SBAR();
  MMA_V(acc[1][1][1], cat(u10_1_0, u10_1_1), cf[1][0]); const s16x4 u12_2_1 = ldtr(o.wb[1][2] + 51200); dma_go<true, 0>(sw5_0, c.voff, o.srdW); SBAR();
  MMA_V(acc[1][2][0], cat(u10_2_0, u10_2_1), cf[0][0]); const s16x4 u12_3_0 = ldtr(o.wb[1][3] + 49152); const unsigned x0_0_3 = cvt_asm(acc[0][1][0][2], acc[0][1][0][3]); SBAR();
  MMA_V(acc[1][2][1], cat(u10_2_0, u10_2_1), cf[1][0]); const s16x4 u12_3_1 = ldtr(o.wb[1][3] + 51200); SBAR();
  LGKM(5); MMA_V(acc[1][3][0], cat(u10_3_0, u10_3_1), cf[0][0]); const s16x4 u13_0_0 = ldtr(o.wb[1][0] + 53248); SBAR();
  MMA_V(acc[1][3][1], cat(u10_3_0, u10_3_1), cf[1][0]); const s16x4 u13_0_1 = ldtr(o.wb[1][0] + 55296); dma_go<true, 1024>(sw5_0, c.voff, o.srdW); SBAR();
  MMA_V(acc[1][0][0], cat(u11_0_0, u11_0_1), cf[0][1]); const s16x4 u13_1_0 = ldtr(o.wb[1][1] + 53248); SBAR();
  MMA_V(acc[1][0][1], cat(u11_0_0, u11_0_1), cf[1][1]); const s16x4 u13_1_1 = ldtr(o.wb[1][1] + 55296); SBAR();
  MMA_V(acc[1][1][0], cat(u11_1_0, u11_1_1), cf[0][1]); const s16x4 u13_2_0 = ldtr(o.wb[1][2] + 53248); SBAR();
  MMA_V(acc[1][1][1], cat(u11_1_0, u11_1_1), cf[1][1]); const s16x4 u13_2_1 = ldtr(o.wb[1][2] + 55296); dma_go<true, 2048>(sw5_0, c.voff, o.srdW); SBAR();
  MMA_V(acc[1][2][0], cat(u11_2_0, u11_2_1), cf[0][1]); const s16x4 u13_3_0 = ldtr(o.wb[1][3] + 53248); SBAR();
  MMA_V(acc[1][2][1], cat(u11_2_0, u11_2_1), cf[1][1]); const s16x4 u13_3_1 = ldtr(o.wb[1][3] + 55296); SBAR();
  MMA_V(acc[1][3][0], cat(u11_3_0, u11_3_1), cf[0][1]); const s16x4 u14_0_0 = ldtr(o.wb[1][0] + 57344); SBAR();
  MMA_V(acc[1][3][1], cat(u11_3_0, u11_3_1), cf[1][1]); const s16x4 u14_0_1 = ldtr(o.wb[1][0] + 59392); const u32x4 x0_0 = {x0_0_0, x0_0_1, x0_0_2, x0_0_3}; st16_sc1(x0_0, c.voff + 0, o.srdX, o.xo + 0); SBAR();
  MMA_V(acc[1][0][0], cat(u12_0_0, u12_0_1), cf[0][2]); const s16x4 u14_1_0 = ldtr(o.wb[1][1] + 57344); const unsigned x0_1_0 = cvt_asm(acc[0][2][0][0], acc[0][2][0][1]); SBAR();
  MMA_V(acc[1][0][1], cat(u12_0_0, u12_0_1), cf[1][2]); const s16x4 u14_1_1 = ldtr(o.wb[1][1] + 59392); const unsigned x0_1_1 = cvt_asm(acc[0][2][0][2], acc[0][2][0][3]); KEEP(x0_0); SBAR();
  LGKM(5); MMA_V(acc[1][1][0], cat(u12_1_0, u12_1_1), cf[0][2]); const s16x4 u14_2_0 = ldtr(o.wb[1][2] + 57344); const unsigned x0_1_2 = cvt_asm(acc[0][3][0][0], acc[0][3][0][1]); SBAR();
  MMA_V(acc[1][1][1], cat(u12_1_0, u12_1_1), cf[1][2]); const s16x4 u14_2_1 = ldtr(o.wb[1][2] + 59392); dma_go<true, 3072>(sw5_0, c.voff, o.srdW); SBAR();
  MMA_V(acc[1][2][0], cat(u12_2_0, u12_2_1), cf[0][2]); const s16x4 u14_3_0 = ldtr(o.wb[1][3] + 57344); const unsigned x0_1_3 = cvt_asm(acc[0][3][0][2], acc[0][3][0][3]); SBAR();
  MMA_V(acc[1][2][1], cat(u12_2_0, u12_2_1), cf[1][2]); const s16x4 u14_3_1 = ldtr(o.wb[1][3] + 59392); SBAR();
  MMA_V(acc[1][3][0], cat(u12_3_0, u12_3_1), cf[0][2]); const s16x4 u15_0_0 = ldtr(o.wb[1][0] + 61440); const unsigned sw5_1 = dma_prep<69632, 167936>(c.m0); SBAR();
  MMA_V(acc[1][3][1], cat(u12_3_0, u12_3_1), cf[1][2]); const s16x4 u15_0_1 = ldtr(o.wb[1][0] + 63488); dma_go<true, 0>(sw5_1, c.voff, o.srdW); SBAR();
  MMA_V(acc[1][0][0], cat(u13_0_0, u13_0_1), cf[0][3]); const s16x4 u15_1_0 = ldtr(o.wb[1][1] + 61440); SBAR();
  MMA_V(acc[1][0][1], cat(u13_0_0, u13_0_1), cf[1][3]); const s16x4 u15_1_1 = ldtr(o.wb[1][1] + 63488); SBAR();
  MMA_V(acc[1][1][0], cat(u13_1_0, u13_1_1), cf[0][3]); const s16x4 u15_2_0 = ldtr(o.wb[1][2] + 61440); SBAR();
  MMA_V(acc[1][1][1], cat(u13_1_0, u13_1_1), cf[1][3]); const s16x4 u15_2_1 = ldtr(o.wb[1][2] + 63488); dma_go<true, 1024>(sw5_1, c.voff, o.srdW); SBAR();
  MMA_V(acc[1][2][0], cat(u13_2_0, u13_2_1), cf[0][3]); const s16x4 u15_3_0 = ldtr(o.wb[1][3] + 61440); SBAR();
  MMA_V(acc[1][2][1], cat(u13_2_0, u13_2_1), cf[1][3]); const s16x4 u15_3_1 = ldtr(o.wb[1][3] + 63488); SBAR();
  LGKM(5); MMA_V(acc[1][3][0], cat(u13_3_0, u13_3_1), cf[0][3]); SBAR();
  MMA_V(acc[1][3][1], cat(u13_3_0, u13_3_1), cf[1][3]); const u32x4 x0_1 = {x0_1_0, x0_1_1, x0_1_2, x0_1_3}; st16_sc1(x0_1, c.voff + 1024, o.srdX, o.xo + 0); const unsigned x0_2_0 = cvt_asm(acc[0][0][1][0], acc[0][0][1][1]); SBAR();
  MMA_V(acc[1][0][0], cat(u14_0_0, u14_0_1), cf[0][4]); const unsigned x0_2_1 = cvt_asm(acc[0][0][1][2], acc[0][0][1][3]); const unsigned x0_2_2 = cvt_asm(acc[0][1][1][0], acc[0][1][1][1]); SBAR();
  MMA_V(acc[1][0][1], cat(u14_0_0, u14_0_1), cf[1][4]); const unsigned x0_2_3 = cvt_asm(acc[0][1][1][2], acc[0][1][1][3]); KEEP(x0_1); SBAR();
  MMA_V(acc[1][1][0], cat(u14_1_0, u14_1_1), cf[0][4]); LGKM(0); asm volatile("s_waitcnt vmcnt(24)\n\ts_barrier" ::: ACLOB, "memory"); SBAR();
  MMA_V(acc[1][1][1], cat(u14_1_0, u14_1_1), cf[1][4]); const s16x4 u16_0_0 = ldtr(o.wb[2][0] + 0); dma_go<true, 2048>(sw5_1, c.voff, o.srdW); SBAR();
  MMA_V(acc[1][2][0], cat(u14_2_0, u14_2_1), cf[0][4]); const s16x4 u16_0_1 = ldtr(o.wb[2][0] + 2048); SBAR();
  MMA_V(acc[1][2][1], cat(u14_2_0, u14_2_1), cf[1][4]); const s16x4 u16_1_0 = ldtr(o.wb[2][1] + 0); const s16x4 u16_1_1 = ldtr(o.wb[2][1] + 2048); SBAR();
  MMA_V(acc[1][3][0], cat(u14_3_0, u14_3_1), cf[0][4]); const s16x4 u16_2_0 = ldtr(o.wb[2][2] + 0); SBAR();
  MMA_V(acc[1][3][1], cat(u14_3_0, u14_3_1), cf[1][4]); const s16x4 u16_2_1 = ldtr(o.wb[2][2] + 2048); dma_go<true, 3072>(sw5_1, c.voff, o.srdW); SBAR();
  MMA_V(acc[1][0][0], cat(u15_0_0, u15_0_1), cf[0][5]); const s16x4 u16_3_0 = ldtr(o.wb[2][3] + 0); SBAR();
  MMA_V(acc[1][0][1], cat(u15_0_0, u15_0_1), cf[1][5]); const s16x4 u16_3_1 = ldtr(o.wb[2][3] + 2048); const s16x4 u17_0_0 = ldtr(o.wb[2][0] + 4096); SBAR();
  MMA_V(acc[1][1][0], cat(u15_1_0, u15_1_1), cf[0][5]); const s16x4 u17_0_1 = ldtr(o.wb[2][0] + 6144); const unsigned sw6_0 = dma_prep<98304, 196608>(c.m0); SBAR();
  MMA_V(acc[1][1][1], cat(u15_1_0, u15_1_1), cf[1][5]); const s16x4 u17_1_0 = ldtr(o.wb[2][1] + 4096); dma_go<true, 0>(sw6_0, c.voff, o.srdW); SBAR();
  MMA_V(acc[1][2][0], cat(u15_2_0, u15_2_1), cf[0][5]); const s16x4 u17_1_1 = ldtr(o.wb[2][1] + 6144); SBAR();
  MMA_V(acc[1][2][1], cat(u15_2_0, u15_2_1), cf[1][5]); const s16x4 u17_2_0 = ldtr(o.wb[2][2] + 4096); const s16x4 u17_2_1 = ldtr(o.wb[2][2] + 6144); SBAR();
  MMA_V(acc[1][3][0], cat(u15_3_0, u15_3_1), cf[0][5]); const s16x4 u17_3_0 = ldtr(o.wb[2][3] + 4096); SBAR();
  MMA_V(acc[1][3][1], cat(u15_3_0, u15_3_1), cf[1][5]); const s16x4 u17_3_1 = ldtr(o.wb[2][3] + 6144); const u32x4 x0_2 = {x0_2_0, x0_2_1, x0_2_2, x0_2_3}; st16_sc1(x0_2, c.voff + 2048, o.srdX, o.xo + 0); SBAR();
  LGKM(6); MMA_V(acc[1][0][0], cat(u16_0_0, u16_0_1), cf[0][6]); const s16x4 u18_0_0 = ldtr(o.wb[2][0] + 8192); const unsigned x0_3_0 = cvt_asm(acc[0][2][1][0], acc[0][2][1][1]); SBAR();
  MMA_V(acc[1][0][1], cat(u16_0_0, u16_0_1), cf[1][6]); const s16x4 u18_0_1 = ldtr(o.wb[2][0] + 10240); const s16x4 u18_1_0 = ldtr(o.wb[2][1] + 8192); KEEP(x0_2); SBAR();
  MMA_V(acc[1][1][0], cat(u16_1_0, u16_1_1), cf[0][6]); const s16x4 u18_1_1 = ldtr(o.wb[2][1] + 10240); const unsigned x0_3_1 = cvt_asm(acc[0][2][1][2], acc[0][2][1][3]); SBAR();
  MMA_V(acc[1][1][1], cat(u16_1_0, u16_1_1), cf[1][6]); const s16x4 u18_2_0 = ldtr(o.wb[2][2] + 8192); dma_go<true, 1024>(sw6_0, c.voff, o.srdW); SBAR();
  MMA_V(acc[1][2][0], cat(u16_2_0, u16_2_1), cf[0][6]); const s16x4 u18_2_1 = ldtr(o.wb[2][2] + 10240); const unsigned x0_3_2 = cvt_asm(acc[0][3][1][0], acc[0][3][1][1]); SBAR();
  MMA_V(acc[1][2][1], cat(u16_2_0, u16_2_1), cf[1][6]); const s16x4 u18_3_0 = ldtr(o.wb[2][3] + 8192); const s16x4 u18_3_1 = ldtr(o.wb[2][3] + 10240); SBAR();
  MMA_V(acc[1][3][0], cat(u16_3_0, u16_3_1), cf[0][6]); const s16x4 u19_0_0 = ldtr(o.wb[2][0] + 12288); const unsigned x0_3_3 = cvt_asm(acc[0][3][1][2], acc[0][3][1][3]); SBAR();
  MMA_V(acc[1][3][1], cat(u16_3_0, u16_3_1), cf[1][6]); const s16x4 u19_0_1 = ldtr(o.wb[2][0] + 14336); dma_go<true, 2048>(sw6_0, c.voff, o.srdW); SBAR();
  MMA_V(acc[1][0][0], cat(u17_0_0, u17_0_1), cf[0][7]); const s16x4 u19_1_0 = ldtr(o.wb[2][1] + 12288); SBAR();
  MMA_V(acc[1][0][1], cat(u17_0_0, u17_0_1), cf[1][7]); const s16x4 u19_1_1 = ldtr(o.wb[2][1] + 14336); SBAR();
  LGKM(6); MMA_V(acc[1][1][0], cat(u17_1_0, u17_1_1), cf[0][7]); const s16x4 u19_2_0 = ldtr(o.wb[2][2] + 12288); SBAR();
  MMA_V(acc[1][1][1], cat(u17_1_0, u17_1_1), cf[1][7]); const s16x4 u19_2_1 = ldtr(o.wb[2][2] + 14336); dma_go<true, 3072>(sw6_0, c.voff, o.srdW); SBAR();
  MMA_V(acc[1][2][0], cat(u17_2_0, u17_2_1), cf[0][7]); const s16x4 u19_3_0 = ldtr(o.wb[2][3] + 12288); SBAR();
  MMA_V(acc[1][2][1], cat(u17_2_0, u17_2_1), cf[1][7]); const s16x4 u19_3_1 = ldtr(o.wb[2][3] + 14336); SBAR();
  MMA_V(acc[1][3][0], cat(u17_3_0, u17_3_1), cf[0][7]); const s16x4 u20_0_0 = ldtr(o.wb[2][0] + 16384); const unsigned sw6_1 = dma_prep<102400, 200704>(c.m0); SBAR();
  MMA_V(acc[1][3][1], cat(u17_3_0, u17_3_1), cf[1][7]); const s16x4 u20_0_1 = ldtr(o.wb[2][0] + 18432); const u32x4 x0_3 = {x0_3_0, x0_3_1, x0_3_2, x0_3_3}; st16_sc1(x0_3, c.voff + 3072, o.srdX, o.xo + 0); SBAR();
  MMA_V0(acc[0][0][0], cat(u18_0_0, u18_0_1), cf[0][8]); const s16x4 u20_1_0 = ldtr(o.wb[2][1] + 16384); SBAR();
  MMA_V0(acc[0][0][1], cat(u18_0_0, u18_0_1), cf[1][8]); const s16x4 u20_1_1 = ldtr(o.wb[2][1] + 18432); KEEP(x0_3); SBAR();
  MMA_V0(acc[0][1][0], cat(u18_1_0, u18_1_1), cf[0][8]); const s16x4 u20_2_0 = ldtr(o.wb[2][2] + 16384); SBAR();
  MMA_V0(acc[0][1][1], cat(u18_1_0, u18_1_1), cf[1][8]); const s16x4 u20_2_1 = ldtr(o.wb[2][2] + 18432); dma_go<true, 0>(sw6_1, c.voff, o.srdW); SBAR();
  MMA_V0(acc[0][2][0], cat(u18_2_0, u18_2_1), cf[0][8]); const s16x4 u20_3_0 = ldtr(o.wb[2][3] + 16384); SBAR();
  MMA_V0(acc[0][2][1], cat(u18_2_0, u18_2_1), cf[1][8]); const s16x4 u20_3_1 = ldtr(o.wb[2][3] + 18432); SBAR();
  LGKM(5); MMA_V0(acc[0][3][0], cat(u18_3_0, u18_3_1), cf[0][8]); const s16x4 u21_0_0 = ldtr(o.wb[2][0] + 20480); const unsigned x1_0_0 = cvt_asm(acc[1][0][0][0], acc[1][0][0][1]); SBAR();
  MMA_V0(acc[0][3][1], cat(u18_3_0, u18_3_1), cf[1][8]); const s16x4 u21_0_1 = ldtr(o.wb[2][0] + 22528); dma_go<true, 1024>(sw6_1, c.voff, o.srdW); SBAR();
  MMA_V(acc[0][0][0], cat(u19_0_0, u19_0_1), cf[0][0]); const s16x4 u21_1_0 = ldtr(o.wb[2][1] + 20480); const unsigned x1_0_1 = cvt_asm(acc[1][0][0][2], acc[1][0][0][3]); SBAR();
  MMA_V(acc[0][0][1], cat(u19_0_0, u19_0_1), cf[1][0]); const s16x4 u21_1_1 = ldtr(o.wb[2][1] + 22528); const unsigned x1_0_2 = cvt_asm(acc[1][1][0][0], acc[1][1][0][1]); SBAR();
  MMA_V(acc[0][1][0], cat(u19_1_0, u19_1_1), cf[0][0]); const s16x4 u21_2_0 = ldtr(o.wb[2][2] + 20480); const unsigned x1_0_3 = cvt_asm(acc[1][1][0][2], acc[1][1][0][3]); SBAR();
  MMA_V(acc[0][1][1], cat(u19_1_0, u19_1_1), cf[1][0]); const s16x4 u21_2_1 = ldtr(o.wb[2][2] + 22528); dma_go<true, 2048>(sw6_1, c.voff, o.srdW); SBAR();
  MMA_V(acc[0][2][0], cat(u19_2_0, u19_2_1), cf[0][0]); const s16x4 u21_3_0 = ldtr(o.wb[2][3] + 20480); SBAR();
  MMA_V(acc[0][2][1], cat(u19_2_0, u19_2_1), cf[1][0]); const s16x4 u21_3_1 = ldtr(o.wb[2][3] + 22528); SBAR();
  MMA_V(acc[0][3][0], cat(u19_3_0, u19_3_1), cf[0][0]); const s16x4 u22_0_0 = ldtr(o.wb[2][0] + 24576); SBAR();
  MMA_V(acc[0][3][1], cat(u19_3_0, u19_3_1), cf[1][0]); const s16x4 u22_0_1 = ldtr(o.wb[2][0] + 26624); const u32x4 x1_0 = {x1_0_0, x1_0_1, x1_0_2, x1_0_3}; st16_sc1(x1_0, c.voff + 0, o.srdX, o.xo + 4096); SBAR();
  MMA_V(acc[0][0][0], cat(u20_0_0, u20_0_1), cf[0][1]); const s16x4 u22_1_0 = ldtr(o.wb[2][1] + 24576); const unsigned x1_1_0 = cvt_asm(acc[1][2][0][0], acc[1][2][0][1]); SBAR();
  MMA_V(acc[0][0][1], cat(u20_0_0, u20_0_1), cf[1][1]); const s16x4 u22_1_1 = ldtr(o.wb[2][1] + 26624); const unsigned x1_1_1 = cvt_asm(acc[1][2][0][2], acc[1][2][0][3]); KEEP(x1_0); SBAR();
  LGKM(5); MMA_V(acc[0][1][0], cat(u20_1_0, u20_1_1), cf[0][1]); const s16x4 u22_2_0 = ldtr(o.wb[2][2] + 24576); const unsigned x1_1_2 = cvt_asm(acc[1][3][0][0], acc[1][3][0][1]); SBAR();
  MMA_V(acc[0][1][1], cat(u20_1_0, u20_1_1), cf[1][1]); const s16x4 u22_2_1 = ldtr(o.wb[2][2] + 26624); dma_go<true, 3072>(sw6_1, c.voff, o.srdW); SBAR();
  MMA_V(acc[0][2][0], cat(u20_2_0, u20_2_1), cf[0][1]); const s16x4 u22_3_0 = ldtr(o.wb[2][3] + 24576); const unsigned x1_1_3 = cvt_asm(acc[1][3][0][2], acc[1][3][0][3]); SBAR();
  MMA_V(acc[0][2][1], cat(u20_2_0, u20_2_1), cf[1][1]); const s16x4 u22_3_1 = ldtr(o.wb[2][3] + 26624); SBAR();
  MMA_V(acc[0][3][0], cat(u20_3_0, u20_3_1), cf[0][1]); const s16x4 u23_0_0 = ldtr(o.wb[2][0] + 28672); SBAR();
  MMA_V(acc[0][3][1], cat(u20_3_0, u20_3_1), cf[1][1]); const s16x4 u23_0_1 = ldtr(o.wb[2][0] + 30720); SBAR();
  MMA_V(acc[0][0][0], cat(u21_0_0, u21_0_1), cf[0][2]); const s16x4 u23_1_0 = ldtr(o.wb[2][1] + 28672); SBAR();
  MMA_V(acc[0][0][1], cat(u21_0_0, u21_0_1), cf[1][2]); const s16x4 u23_1_1 = ldtr(o.wb[2][1] + 30720); SBAR();
  MMA_V(acc[0][1][0], cat(u21_1_0, u21_1_1), cf[0][2]); const s16x4 u23_2_0 = ldtr(o.wb[2][2] + 28672); SBAR();
  MMA_V(acc[0][1][1], cat(u21_1_0, u21_1_1), cf[1][2]); const s16x4 u23_2_1 = ldtr(o.wb[2][2] + 30720); SBAR();
  MMA_V(acc[0][2][0], cat(u21_2_0, u21_2_1), cf[0][2]); const s16x4 u23_3_0 = ldtr(o.wb[2][3] + 28672); SBAR();
  MMA_V(acc[0][2][1], cat(u21_2_0, u21_2_1), cf[1][2]); const s16x4 u23_3_1 = ldtr(o.wb[2][3] + 30720); SBAR();
  LGKM(5); MMA_V(acc[0][3][0], cat(u21_3_0, u21_3_1), cf[0][2]); SBAR();
  MMA_V(acc[0][3][1], cat(u21_3_0, u21_3_1), cf[1][2]); const u32x4 x1_1 = {x1_1_0, x1_1_1, x1_1_2, x1_1_3}; st16_sc1(x1_1, c.voff + 1024, o.srdX, o.xo + 4096); const unsigned x1_2_0 = cvt_asm(acc[1][0][1][0], acc[1][0][1][1]); SBAR();
  MMA_V(acc[0][0][0], cat(u22_0_0, u22_0_1), cf[0][3]); const unsigned x1_2_1 = cvt_asm(acc[1][0][1][2], acc[1][0][1][3]); const unsigned x1_2_2 = cvt_asm(acc[1][1][1][0], acc[1][1][1][1]); SBAR();
  MMA_V(acc[0][0][1], cat(u22_0_0, u22_0_1), cf[1][3]); const unsigned x1_2_3 = cvt_asm(acc[1][1][1][2], acc[1][1][1][3]); KEEP(x1_1); SBAR();
  MMA_V(acc[0][1][0], cat(u22_1_0, u22_1_1), cf[0][3]); LGKM(0); asm volatile("s_waitcnt vmcnt(30)\n\ts_barrier" ::: ACLOB, "memory"); SBAR();
  MMA_V(acc[0][1][1], cat(u22_1_0, u22_1_1), cf[1][3]); const s16x4 u24_0_0 = ldtr(o.wb[0][0] + 0); const unsigned sw7_0 = dma_prep<131072, 229376>(c.m0); dma_go<true, 0>(sw7_0, c.voff, o.srdW); SBAR();
  MMA_V(acc[0][2][0], cat(u22_2_0, u22_2_1), cf[0][3]); const s16x4 u24_0_1 = ldtr(o.wb[0][0] + 2048); SBAR();
  MMA_V(acc[0][2][1], cat(u22_2_0, u22_2_1), cf[1][3]); const s16x4 u24_1_0 = ldtr(o.wb[0][1] + 0); const s16x4 u24_1_1 = ldtr(o.wb[0][1] + 2048); SBAR();
  MMA_V(acc[0][3][0], cat(u22_3_0, u22_3_1), cf[0][3]); const s16x4 u24_2_0 = ldtr(o.wb[0][2] + 0); SBAR();
  MMA_V(acc[0][3][1], cat(u22_3_0, u22_3_1), cf[1][3]); const s16x4 u24_2_1 = ldtr(o.wb[0][2] + 2048); dma_go<true, 1024>(sw7_0, c.voff, o.srdW); SBAR();
  MMA_V(acc[0][0][0], cat(u23_0_0, u23_0_1), cf[0][4]); const s16x4 u24_3_0 = ldtr(o.wb[0][3] + 0); SBAR();
  MMA_V(acc[0][0][1], cat(u23_0_0, u23_0_1), cf[1][4]); const s16x4 u24_3_1 = ldtr(o.wb[0][3] + 2048); const s16x4 u25_0_0 = ldtr(o.wb[0][0] + 4096); SBAR();
  MMA_V(acc[0][1][0], cat(u23_1_0, u23_1_1), cf[0][4]); const s16x4 u25_0_1 = ldtr(o.wb[0][0] + 6144); SBAR();
  MMA_V(acc[0][1][1], cat(u23_1_0, u23_1_1), cf[1][4]); const s16x4 u25_1_0 = ldtr(o.wb[0][1] + 4096); dma_go<true, 2048>(sw7_0, c.voff, o.srdW); SBAR();
  MMA_V(acc[0][2][0], cat(u23_2_0, u23_2_1), cf[0][4]); const s16x4 u25_1_1 = ldtr(o.wb[0][1] + 6144); SBAR();
  MMA_V(acc[0][2][1], cat(u23_2_0, u23_2_1), cf[1][4]); const s16x4 u25_2_0 = ldtr(o.wb[0][2] + 4096); const s16x4 u25_2_1 = ldtr(o.wb[0][2] + 6144); SBAR();
  MMA_V(acc[0][3][0], cat(u23_3_0, u23_3_1), cf[0][4]); const s16x4 u25_3_0 = ldtr(o.wb[0][3] + 4096); SBAR();
  MMA_V(acc[0][3][1], cat(u23_3_0, u23_3_1), cf[1][4]); const s16x4 u25_3_1 = ldtr(o.wb[0][3] + 6144); const u32x4 x1_2 = {x1_2_0, x1_2_1, x1_2_2, x1_2_3}; st16_sc1(x1_2, c.voff + 2048, o.srdX, o.xo + 4096); SBAR();
  LGKM(6); MMA_V(acc[0][0][0], cat(u24_0_0, u24_0_1), cf[0][5]); const s16x4 u26_0_0 = ldtr(o.wb[0][0] + 8192); const unsigned x1_3_0 = cvt_asm(acc[1][2][1][0], acc[1][2][1][1]); SBAR();
  MMA_V(acc[0][0][1], cat(u24_0_0, u24_0_1), cf[1][5]); const s16x4 u26_0_1 = ldtr(o.wb[0][0] + 10240); const s16x4 u26_1_0 = ldtr(o.wb[0][1] + 8192); KEEP(x1_2); SBAR();
  MMA_V(acc[0][1][0], cat(u24_1_0, u24_1_1), cf[0][5]); const s16x4 u26_1_1 = ldtr(o.wb[0][1] + 10240); const unsigned x1_3_1 = cvt_asm(acc[1][2][1][2], acc[1][2][1][3]); SBAR();
  MMA_V(acc[0][1][1], cat(u24_1_0, u24_1_1), cf[1][5]); const s16x4 u26_2_0 = ldtr(o.wb[0][2] + 8192); dma_go<true, 3072>(sw7_0, c.voff, o.srdW); SBAR();
  MMA_V(acc[0][2][0], cat(u24_2_0, u24_2_1), cf[0][5]); const s16x4 u26_2_1 = ldtr(o.wb[0][2] + 10240); const unsigned x1_3_2 = cvt_asm(acc[1][3][1][0], acc[1][3][1][1]); SBAR();
  MMA_V(acc[0][2][1], cat(u24_2_0, u24_2_1), cf[1][5]); const s16x4 u26_3_0 = ldtr(o.wb[0][3] + 8192); const s16x4 u26_3_1 = ldtr(o.wb[0][3] + 10240); SBAR();
  MMA_V(acc[0][3][0], cat(u24_3_0, u24_3_1), cf[0][5]); const s16x4 u27_0_0 = ldtr(o.wb[0][0] + 12288); const unsigned sw7_1 = dma_prep<135168, 233472>(c.m0); SBAR();
  MMA_V(acc[0][3][1], cat(u24_3_0, u24_3_1), cf[1][5]); const s16x4 u27_0_1 = ldtr(o.wb[0][0] + 14336); dma_go<true, 0>(sw7_1, c.voff, o.srdW); SBAR();
  MMA_V(acc[0][0][0], cat(u25_0_0, u25_0_1), cf[0][6]); const s16x4 u27_1_0 = ldtr(o.wb[0][1] + 12288); const unsigned x1_3_3 = cvt_asm(acc[1][3][1][2], acc[1][3][1][3]); SBAR();
  MMA_V(acc[0][0][1], cat(u25_0_0, u25_0_1), cf[1][6]); const s16x4 u27_1_1 = ldtr(o.wb[0][1] + 14336); SBAR();
  LGKM(6); MMA_V(acc[0][1][0], cat(u25_1_0, u25_1_1), cf[0][6]); const s16x4 u27_2_0 = ldtr(o.wb[0][2] + 12288); SBAR();
  MMA_V(acc[0][1][1], cat(u25_1_0, u25_1_1), cf[1][6]); const s16x4 u27_2_1 = ldtr(o.wb[0][2] + 14336); dma_go<true, 1024>(sw7_1, c.voff, o.srdW); SBAR();
  MMA_V(acc[0][2][0], cat(u25_2_0, u25_2_1), cf[0][6]); const s16x4 u27_3_0 = ldtr(o.wb[0][3] + 12288); SBAR();
  MMA_V(acc[0][2][1], cat(u25_2_0, u25_2_1), cf[1][6]); const s16x4 u27_3_1 = ldtr(o.wb[0][3] + 14336); SBAR();
  MMA_V(acc[0][3][0], cat(u25_3_0, u25_3_1), cf[0][6]); const s16x4 u28_0_0 = ldtr(o.wb[0][0] + 16384); SBAR();
  MMA_V(acc[0][3][1], cat(u25_3_0, u25_3_1), cf[1][6]); const s16x4 u28_0_1 = ldtr(o.wb[0][0] + 18432); const u32x4 x1_3 = {x1_3_0, x1_3_1, x1_3_2, x1_3_3}; st16_sc1(x1_3, c.voff + 3072, o.srdX, o.xo + 4096); SBAR();
  MMA_V(acc[0][0][0], cat(u26_0_0, u26_0_1), cf[0][7]); const s16x4 u28_1_0 = ldtr(o.wb[0][1] + 16384); SBAR();
  MMA_V(acc[0][0][1], cat(u26_0_0, u26_0_1), cf[1][7]); const s16x4 u28_1_1 = ldtr(o.wb[0][1] + 18432); KEEP(x1_3); SBAR();
  MMA_V(acc[0][1][0], cat(u26_1_0, u26_1_1), cf[0][7]); const s16x4 u28_2_0 = ldtr(o.wb[0][2] + 16384); SBAR();
  MMA_V(acc[0][1][1], cat(u26_1_0, u26_1_1), cf[1][7]); const s16x4 u28_2_1 = ldtr(o.wb[0][2] + 18432); dma_go<true, 2048>(sw7_1, c.voff, o.srdW); SBAR();
  MMA_V(acc[0][2][0], cat(u26_2_0, u26_2_1), cf[0][7]); const s16x4 u28_3_0 = ldtr(o.wb[0][3] + 16384); SBAR();
  MMA_V(acc[0][2][1], cat(u26_2_0, u26_2_1), cf[1][7]); const s16x4 u28_3_1 = ldtr(o.wb[0][3] + 18432); SBAR();
  LGKM(5); MMA_V(acc[0][3][0], cat(u26_3_0, u26_3_1), cf[0][7]); const s16x4 u29_0_0 = ldtr(o.wb[0][0] + 20480); SBAR();
  MMA_V(acc[0][3][1], cat(u26_3_0, u26_3_1), cf[1][7]); const s16x4 u29_0_1 = ldtr(o.wb[0][0] + 22528); dma_go<true, 3072>(sw7_1, c.voff, o.srdW); SBAR();
  MMA_V0(acc[1][0][0], cat(u27_0_0, u27_0_1), cf[0][8]); const s16x4 u29_1_0 = ldtr(o.wb[0][1] + 20480); SBAR();
  MMA_V0(acc[1][0][1], cat(u27_0_0, u27_0_1), cf[1][8]); const s16x4 u29_1_1 = ldtr(o.wb[0][1] + 22528); SBAR();
  MMA_V0(acc[1][1][0], cat(u27_1_0, u27_1_1), cf[0][8]); const s16x4 u29_2_0 = ldtr(o.wb[0][2] + 20480); SBAR();
  MMA_V0(acc[1][1][1], cat(u27_1_0, u27_1_1), cf[1][8]); const s16x4 u29_2_1 = ldtr(o.wb[0][2] + 22528); SBAR();
  MMA_V0(acc[1][2][0], cat(u27_2_0, u27_2_1), cf[0][8]); const s16x4 u29_3_0 = ldtr(o.wb[0][3] + 20480); SBAR();
  MMA_V0(acc[1][2][1], cat(u27_2_0, u27_2_1), cf[1][8]); const s16x4 u29_3_1 = ldtr(o.wb[0][3] + 22528); SBAR();
  MMA_V0(acc[1][3][0], cat(u27_3_0, u27_3_1), cf[0][8]); const s16x4 u30_0_0 = ldtr(o.wb[0][0] + 24576); const unsigned x2_0_0 = cvt_asm(acc[0][0][0][0], acc[0][0][0][1]); SBAR();
  MMA_V0(acc[1][3][1], cat(u27_3_0, u27_3_1), cf[1][8]); const s16x4 u30_0_1 = ldtr(o.wb[0][0] + 26624); const unsigned x2_0_1 = cvt_asm(acc[0][0][0][2], acc[0][0][0][3]); SBAR();
  MMA_V(acc[1][0][0], cat(u28_0_0, u28_0_1), cf[0][0]); const s16x4 u30_1_0 = ldtr(o.wb[0][1] + 24576); const unsigned x2_0_2 = cvt_asm(acc[0][1][0][0], acc[0][1][0][1]); SBAR();
  MMA_V(acc[1][0][1], cat(u28_0_0, u28_0_1), cf[1][0]); const s16x4 u30_1_1 = ldtr(o.wb[0][1] + 26624); const unsigned x2_0_3 = cvt_asm(acc[0][1][0][2], acc[0][1][0][3]); SBAR();
  LGKM(5); MMA_V(acc[1][1][0], cat(u28_1_0, u28_1_1), cf[0][0]); const s16x4 u30_2_0 = ldtr(o.wb[0][2] + 24576); SBAR();
  MMA_V(acc[1][1][1], cat(u28_1_0, u28_1_1), cf[1][0]); const s16x4 u30_2_1 = ldtr(o.wb[0][2] + 26624); SBAR();
  MMA_V(acc[1][2][0], cat(u28_2_0, u28_2_1), cf[0][0]); const s16x4 u30_3_0 = ldtr(o.wb[0][3] + 24576); SBAR();
  MMA_V(acc[1][2][1], cat(u28_2_0, u28_2_1), cf[1][0]); const s16x4 u30_3_1 = ldtr(o.wb[0][3] + 26624); SBAR();
  MMA_V(acc[1][3][0], cat(u28_3_0, u28_3_1), cf[0][0]); const s16x4 u31_0_0 = ldtr(o.wb[0][0] + 28672); SBAR();
  MMA_V(acc[1][3][1], cat(u28_3_0, u28_3_1), cf[1][0]); const s16x4 u31_0_1 = ldtr(o.wb[0][0] + 30720); SBAR();
  MMA_V(acc[1][0][0], cat(u29_0_0, u29_0_1), cf[0][1]); const s16x4 u31_1_0 = ldtr(o.wb[0][1] + 28672); SBAR();
  MMA_V(acc[1][0][1], cat(u29_0_0, u29_0_1), cf[1][1]); const s16x4 u31_1_1 = ldtr(o.wb[0][1] + 30720); SBAR();
  MMA_V(acc[1][1][0], cat(u29_1_0, u29_1_1), cf[0][1]); const s16x4 u31_2_0 = ldtr(o.wb[0][2] + 28672); SBAR();
  MMA_V(acc[1][1][1], cat(u29_1_0, u29_1_1), cf[1][1]); const s16x4 u31_2_1 = ldtr(o.wb[0][2] + 30720); SBAR();
  MMA_V(acc[1][2][0], cat(u29_2_0, u29_2_1), cf[0][1]); const s16x4 u31_3_0 = ldtr(o.wb[0][3] + 28672); SBAR();
  MMA_V(acc[1][2][1], cat(u29_2_0, u29_2_1), cf[1][1]); const s16x4 u31_3_1 = ldtr(o.wb[0][3] + 30720); SBAR();
  LGKM(5); MMA_V(acc[1][3][0], cat(u29_3_0, u29_3_1), cf[0][1]); SBAR();
  MMA_V(acc[1][3][1], cat(u29_3_0, u29_3_1), cf[1][1]); const u32x4 x2_0 = {x2_0_0, x2_0_1, x2_0_2, x2_0_3}; st16_sc1(x2_0, c.voff + 0, o.srdX, o.xo + 8192); const unsigned x2_1_0 = cvt_asm(acc[0][2][0][0], acc[0][2][0][1]); SBAR();
  MMA_V(acc[1][0][0], cat(u30_0_0, u30_0_1), cf[0][2]); const unsigned x2_1_1 = cvt_asm(acc[0][2][0][2], acc[0][2][0][3]); const unsigned x2_1_2 = cvt_asm(acc[0][3][0][0], acc[0][3][0][1]); SBAR();
  MMA_V(acc[1][0][1], cat(u30_0_0, u30_0_1), cf[1][2]); const unsigned x2_1_3 = cvt_asm(acc[0][3][0][2], acc[0][3][0][3]); KEEP(x2_0); SBAR();
  MMA_V(acc[1][1][0], cat(u30_1_0, u30_1_1), cf[0][2]); LGKM(0); asm volatile("s_waitcnt vmcnt(33)\n\ts_barrier" ::: ACLOB, "memory"); SBAR();
  MMA_V(acc[1][1][1], cat(u30_1_0, u30_1_1), cf[1][2]); const s16x4 u32_0_0 = ldtr(o.wb[0][0] + 32768); SBAR();
  MMA_V(acc[1][2][0], cat(u30_2_0, u30_2_1), cf[0][2]); const s16x4 u32_0_1 = ldtr(o.wb[0][0] + 34816); SBAR();
  MMA_V(acc[1][2][1], cat(u30_2_0, u30_2_1), cf[1][2]); const s16x4 u32_1_0 = ldtr(o.wb[0][1] + 32768); const s16x4 u32_1_1 = ldtr(o.wb[0][1] + 34816); SBAR();
  MMA_V(acc[1][3][0], cat(u30_3_0, u30_3_1), cf[0][2]); const s16x4 u32_2_0 = ldtr(o.wb[0][2] + 32768); SBAR();
  MMA_V(acc[1][3][1], cat(u30_3_0, u30_3_1), cf[1][2]); const s16x4 u32_2_1 = ldtr(o.wb[0][2] + 34816); SBAR();
  MMA_V(acc[1][0][0], cat(u31_0_0, u31_0_1), cf[0][3]); const s16x4 u32_3_0 = ldtr(o.wb[0][3] + 32768); SBAR();
  MMA_V(acc[1][0][1], cat(u31_0_0, u31_0_1), cf[1][3]); const s16x4 u32_3_1 = ldtr(o.wb[0][3] + 34816); const s16x4 u33_0_0 = ldtr(o.wb[0][0] + 36864); SBAR();
  MMA_V(acc[1][1][0], cat(u31_1_0, u31_1_1), cf[0][3]); const s16x4 u33_0_1 = ldtr(o.wb[0][0] + 38912); SBAR();
  MMA_V(acc[1][1][1], cat(u31_1_0, u31_1_1), cf[1][3]); const s16x4 u33_1_0 = ldtr(o.wb[0][1] + 36864); SBAR();
  MMA_V(acc[1][2][0], cat(u31_2_0, u31_2_1), cf[0][3]); const s16x4 u33_1_1 = ldtr(o.wb[0][1] + 38912); SBAR();
  MMA_V(acc[1][2][1], cat(u31_2_0, u31_2_1), cf[1][3]); const s16x4 u33_2_0 = ldtr(o.wb[0][2] + 36864); const s16x4 u33_2_1 = ldtr(o.wb[0][2] + 38912); SBAR();
  MMA_V(acc[1][3][0], cat(u31_3_0, u31_3_1), cf[0][3]); const s16x4 u33_3_0 = ldtr(o.wb[0][3] + 36864); SBAR();
  MMA_V(acc[1][3][1], cat(u31_3_0, u31_3_1), cf[1][3]); const s16x4 u33_3_1 = ldtr(o.wb[0][3] + 38912); const u32x4 x2_1 = {x2_1_0, x2_1_1, x2_1_2, x2_1_3}; st16_sc1(x2_1, c.voff + 1024, o.srdX, o.xo + 8192); SBAR();
  LGKM(6); MMA_V(acc[1][0][0], cat(u32_0_0, u32_0_1), cf[0][4]); const s16x4 u34_0_0 = ldtr(o.wb[0][0] + 40960); const unsigned x2_2_0 = cvt_asm(acc[0][0][1][0], acc[0][0][1][1]); SBAR();
  MMA_V(acc[1][0][1], cat(u32_0_0, u32_0_1), cf[1][4]); const s16x4 u34_0_1 = ldtr(o.wb[0][0] + 43008); const s16x4 u34_1_0 = ldtr(o.wb[0][1] + 40960); KEEP(x2_1); SBAR();
  MMA_V(acc[1][1][0], cat(u32_1_0, u32_1_1), cf[0][4]); const s16x4 u34_1_1 = ldtr(o.wb[0][1] + 43008); const unsigned x2_2_1 = cvt_asm(acc[0][0][1][2], acc[0][0][1][3]); SBAR();
  MMA_V(acc[1][1][1], cat(u32_1_0, u32_1_1), cf[1][4]); const s16x4 u34_2_0 = ldtr(o.wb[0][2] + 40960); const unsigned x2_2_2 = cvt_asm(acc[0][1][1][0], acc[0][1][1][1]); SBAR();
  MMA_V(acc[1][2][0], cat(u32_2_0, u32_2_1), cf[0][4]); const s16x4 u34_2_1 = ldtr(o.wb[0][2] + 43008); const unsigned x2_2_3 = cvt_asm(acc[0][1][1][2], acc[0][1][1][3]); SBAR();
  MMA_V(acc[1][2][1], cat(u32_2_0, u32_2_1), cf[1][4]); const s16x4 u34_3_0 = ldtr(o.wb[0][3] + 40960); const s16x4 u34_3_1 = ldtr(o.wb[0][3] + 43008); SBAR();
  MMA_V(acc[1][3][0], cat(u32_3_0, u32_3_1), cf[0][4]); const s16x4 u35_0_0 = ldtr(o.wb[0][0] + 45056); SBAR();
  MMA_V(acc[1][3][1], cat(u32_3_0, u32_3_1), cf[1][4]); const s16x4 u35_0_1 = ldtr(o.wb[0][0] + 47104); SBAR();
  MMA_V(acc[1][0][0], cat(u33_0_0, u33_0_1), cf[0][5]); const s16x4 u35_1_0 = ldtr(o.wb[0][1] + 45056); SBAR();
  MMA_V(acc[1][0][1], cat(u33_0_0, u33_0_1), cf[1][5]); const s16x4 u35_1_1 = ldtr(o.wb[0][1] + 47104); SBAR();
  LGKM(6); MMA_V(acc[1][1][0], cat(u33_1_0, u33_1_1), cf[0][5]); const s16x4 u35_2_0 = ldtr(o.wb[0][2] + 45056); SBAR();
  MMA_V(acc[1][1][1], cat(u33_1_0, u33_1_1), cf[1][5]); const s16x4 u35_2_1 = ldtr(o.wb[0][2] + 47104); SBAR();
  MMA_V(acc[1][2][0], cat(u33_2_0, u33_2_1), cf[0][5]); const s16x4 u35_3_0 = ldtr(o.wb[0][3] + 45056); SBAR();
  MMA_V(acc[1][2][1], cat(u33_2_0, u33_2_1), cf[1][5]); const s16x4 u35_3_1 = ldtr(o.wb[0][3] + 47104); SBAR();
  MMA_V(acc[1][3][0], cat(u33_3_0, u33_3_1), cf[0][5]); const s16x4 u36_0_0 = ldtr(o.wb[0][0] + 49152); SBAR();
  MMA_V(acc[1][3][1], cat(u33_3_0, u33_3_1), cf[1][5]); const s16x4 u36_0_1 = ldtr(o.wb[0][0] + 51200); const u32x4 x2_2 = {x2_2_0, x2_2_1, x2_2_2, x2_2_3}; st16_sc1(x2_2, c.voff + 2048, o.srdX, o.xo + 8192); SBAR();
  MMA_V(acc[1][0][0], cat(u34_0_0, u34_0_1), cf[0][6]); const s16x4 u36_1_0 = ldtr(o.wb[0][1] + 49152); const unsigned x2_3_0 = cvt_asm(acc[0][2][1][0], acc[0][2][1][1]); SBAR();
  MMA_V(acc[1][0][1], cat(u34_0_0, u34_0_1), cf[1][6]); const s16x4 u36_1_1 = ldtr(o.wb[0][1] + 51200); const unsigned x2_3_1 = cvt_asm(acc[0][2][1][2], acc[0][2][1][3]); KEEP(x2_2); SBAR();
  MMA_V(acc[1][1][0], cat(u34_1_0, u34_1_1), cf[0][6]); const s16x4 u36_2_0 = ldtr(o.wb[0][2] + 49152); const unsigned x2_3_2 = cvt_asm(acc[0][3][1][0], acc[0][3][1][1]); SBAR();
  MMA_V(acc[1][1][1], cat(u34_1_0, u34_1_1), cf[1][6]); const s16x4 u36_2_1 = ldtr(o.wb[0][2] + 51200); const unsigned x2_3_3 = cvt_asm(acc[0][3][1][2], acc[0][3][1][3]); SBAR();
  MMA_V(acc[1][2][0], cat(u34_2_0, u34_2_1), cf[0][6]); const s16x4 u36_3_0 = ldtr(o.wb[0][3] + 49152); SBAR();
  MMA_V(acc[1][2][1], cat(u34_2_0, u34_2_1), cf[1][6]); const s16x4 u36_3_1 = ldtr(o.wb[0][3] + 51200); SBAR();
  LGKM(5); MMA_V(acc[1][3][0], cat(u34_3_0, u34_3_1), cf[0][6]); const s16x4 u37_0_0 = ldtr(o.wb[0][0] + 53248); SBAR();
  MMA_V(acc[1][3][1], cat(u34_3_0, u34_3_1), cf[1][6]); const s16x4 u37_0_1 = ldtr(o.wb[0][0] + 55296); SBAR();
  MMA_V(acc[1][0][0], cat(u35_0_0, u35_0_1), cf[0][7]); const s16x4 u37_1_0 = ldtr(o.wb[0][1] + 53248); SBAR();
  MMA_V(acc[1][0][1], cat(u35_0_0, u35_0_1), cf[1][7]); const s16x4 u37_1_1 = ldtr(o.wb[0][1] + 55296); SBAR();
  MMA_V(acc[1][1][0], cat(u35_1_0, u35_1_1), cf[0][7]); const s16x4 u37_2_0 = ldtr(o.wb[0][2] + 53248); SBAR();
  MMA_V(acc[1][1][1], cat(u35_1_0, u35_1_1), cf[1][7]); const s16x4 u37_2_1 = ldtr(o.wb[0][2] + 55296); SBAR();
  MMA_V(acc[1][2][0], cat(u35_2_0, u35_2_1), cf[0][7]); const s16x4 u37_3_0 = ldtr(o.wb[0][3] + 53248); SBAR();
  MMA_V(acc[1][2][1], cat(u35_2_0, u35_2_1), cf[1][7]); const s16x4 u37_3_1 = ldtr(o.wb[0][3] + 55296); SBAR();
  MMA_V(acc[1][3][0], cat(u35_3_0, u35_3_1), cf[0][7]); const s16x4 u38_0_0 = ldtr(o.wb[0][0] + 57344); SBAR();
  MMA_V(acc[1][3][1], cat(u35_3_0, u35_3_1), cf[1][7]); const s16x4 u38_0_1 = ldtr(o.wb[0][0] + 59392); const u32x4 x2_3 = {x2_3_0, x2_3_1, x2_3_2, x2_3_3}; st16_sc1(x2_3, c.voff + 3072, o.srdX, o.xo + 8192); SBAR();
  MMA_V0(acc[0][0][0], cat(u36_0_0, u36_0_1), cf[0][8]); const s16x4 u38_1_0 = ldtr(o.wb[0][1] + 57344); SBAR();
  MMA_V0(acc[0][0][1], cat(u36_0_0, u36_0_1), cf[1][8]); const s16x4 u38_1_1 = ldtr(o.wb[0][1] + 59392); KEEP(x2_3); SBAR();
  LGKM(5); MMA_V0(acc[0][1][0], cat(u36_1_0, u36_1_1), cf[0][8]); const s16x4 u38_2_0 = ldtr(o.wb[0][2] + 57344); SBAR();
  MMA_V0(acc[0][1][1], cat(u36_1_0, u36_1_1), cf[1][8]); const s16x4 u38_2_1 = ldtr(o.wb[0][2] + 59392); SBAR();
  MMA_V0(acc[0][2][0], cat(u36_2_0, u36_2_1), cf[0][8]); const s16x4 u38_3_0 = ldtr(o.wb[0][3] + 57344); SBAR();
  MMA_V0(acc[0][2][1], cat(u36_2_0, u36_2_1), cf[1][8]); const s16x4 u38_3_1 = ldtr(o.wb[0][3] + 59392); SBAR();
  MMA_V0(acc[0][3][0], cat(u36_3_0, u36_3_1), cf[0][8]); const s16x4 u39_0_0 = ldtr(o.wb[0][0] + 61440); const unsigned x3_0_0 = cvt_asm(acc[1][0][0][0], acc[1][0][0][1]); SBAR();
  MMA_V0(acc[0][3][1], cat(u36_3_0, u36_3_1), cf[1][8]); const s16x4 u39_0_1 = ldtr(o.wb[0][0] + 63488); const unsigned x3_0_1 = cvt_asm(acc[1][0][0][2], acc[1][0][0][3]); SBAR();
  MMA_V(acc[0][0][0], cat(u37_0_0, u37_0_1), cf[0][0]); const s16x4 u39_1_0 = ldtr(o.wb[0][1] + 61440); const unsigned x3_0_2 = cvt_asm(acc[1][1][0][0], acc[1][1][0][1]); SBAR();
  MMA_V(acc[0][0][1], cat(u37_0_0, u37_0_1), cf[1][0]); const s16x4 u39_1_1 = ldtr(o.wb[0][1] + 63488); const unsigned x3_0_3 = cvt_asm(acc[1][1][0][2], acc[1][1][0][3]); SBAR();
  MMA_V(acc[0][1][0], cat(u37_1_0, u37_1_1), cf[0][0]); const s16x4 u39_2_0 = ldtr(o.wb[0][2] + 61440); SBAR();
  MMA_V(acc[0][1][1], cat(u37_1_0, u37_1_1), cf[1][0]); const s16x4 u39_2_1 = ldtr(o.wb[0][2] + 63488); SBAR();
  MMA_V(acc[0][2][0], cat(u37_2_0, u37_2_1), cf[0][0]); const s16x4 u39_3_0 = ldtr(o.wb[0][3] + 61440); SBAR();
  MMA_V(acc[0][2][1], cat(u37_2_0, u37_2_1), cf[1][0]); const s16x4 u39_3_1 = ldtr(o.wb[0][3] + 63488); SBAR();
  LGKM(5); MMA_V(acc[0][3][0], cat(u37_3_0, u37_3_1), cf[0][0]); SBAR();
  MMA_V(acc[0][3][1], cat(u37_3_0, u37_3_1), cf[1][0]); const u32x4 x3_0 = {x3_0_0, x3_0_1, x3_0_2, x3_0_3}; st16_sc1(x3_0, c.voff + 0, o.srdX, o.xo + 12288); const unsigned x3_1_0 = cvt_asm(acc[1][2][0][0], acc[1][2][0][1]); SBAR();
  MMA_V(acc[0][0][0], cat(u38_0_0, u38_0_1), cf[0][1]); const unsigned x3_1_1 = cvt_asm(acc[1][2][0][2], acc[1][2][0][3]); const unsigned x3_1_2 = cvt_asm(acc[1][3][0][0], acc[1][3][0][1]); SBAR();
  MMA_V(acc[0][0][1], cat(u38_0_0, u38_0_1), cf[1][1]); const unsigned x3_1_3 = cvt_asm(acc[1][3][0][2], acc[1][3][0][3]); KEEP(x3_0); SBAR();
  MMA_V(acc[0][1][0], cat(u38_1_0, u38_1_1), cf[0][1]); LGKM(0); asm volatile("s_waitcnt vmcnt(27)\n\ts_barrier" ::: ACLOB, "memory"); SBAR();
  MMA_V(acc[0][1][1], cat(u38_1_0, u38_1_1), cf[1][1]); const s16x4 u40_0_0 = ldtr(o.wb[1][0] + 0); SBAR();
  MMA_V(acc[0][2][0], cat(u38_2_0, u38_2_1), cf[0][1]); const s16x4 u40_0_1 = ldtr(o.wb[1][0] + 2048); SBAR();
  MMA_V(acc[0][2][1], cat(u38_2_0, u38_2_1), cf[1][1]); const s16x4 u40_1_0 = ldtr(o.wb[1][1] + 0); const s16x4 u40_1_1 = ldtr(o.wb[1][1] + 2048); SBAR();
  MMA_V(acc[0][3][0], cat(u38_3_0, u38_3_1), cf[0][1]); const s16x4 u40_2_0 = ldtr(o.wb[1][2] + 0); SBAR();
  MMA_V(acc[0][3][1], cat(u38_3_0, u38_3_1), cf[1][1]); const s16x4 u40_2_1 = ldtr(o.wb[1][2] + 2048); SBAR();
  MMA_V(acc[0][0][0], cat(u39_0_0, u39_0_1), cf[0][2]); const s16x4 u40_3_0 = ldtr(o.wb[1][3] + 0); SBAR();
  MMA_V(acc[0][0][1], cat(u39_0_0, u39_0_1), cf[1][2]); const s16x4 u40_3_1 = ldtr(o.wb[1][3] + 2048); const s16x4 u41_0_0 = ldtr(o.wb[1][0] + 4096); SBAR();
  MMA_V(acc[0][1][0], cat(u39_1_0, u39_1_1), cf[0][2]); const s16x4 u41_0_1 = ldtr(o.wb[1][0] + 6144); SBAR();
  MMA_V(acc[0][1][1], cat(u39_1_0, u39_1_1), cf[1][2]); const s16x4 u41_1_0 = ldtr(o.wb[1][1] + 4096); SBAR();
  MMA_V(acc[0][2][0], cat(u39_2_0, u39_2_1), cf[0][2]); const s16x4 u41_1_1 = ldtr(o.wb[1][1] + 6144); SBAR();
  MMA_V(acc[0][2][1], cat(u39_2_0, u39_2_1), cf[1][2]); const s16x4 u41_2_0 = ldtr(o.wb[1][2] + 4096); const s16x4 u41_2_1 = ldtr(o.wb[1][2] + 6144); SBAR();
  MMA_V(acc[0][3][0], cat(u39_3_0, u39_3_1), cf[0][2]); const s16x4 u41_3_0 = ldtr(o.wb[1][3] + 4096); SBAR();
  MMA_V(acc[0][3][1], cat(u39_3_0, u39_3_1), cf[1][2]); const s16x4 u41_3_1 = ldtr(o.wb[1][3] + 6144); const u32x4 x3_1 = {x3_1_0, x3_1_1, x3_1_2, x3_1_3}; st16_sc1(x3_1, c.voff + 1024, o.srdX, o.xo + 12288); SBAR();
  LGKM(6); MMA_V(acc[0][0][0], cat(u40_0_0, u40_0_1), cf[0][3]); const s16x4 u42_0_0 = ldtr(o.wb[1][0] + 8192); const unsigned x3_2_0 = cvt_asm(acc[1][0][1][0], acc[1][0][1][1]); SBAR();
  MMA_V(acc[0][0][1], cat(u40_0_0, u40_0_1), cf[1][3]); const s16x4 u42_0_1 = ldtr(o.wb[1][0] + 10240); const s16x4 u42_1_0 = ldtr(o.wb[1][1] + 8192); KEEP(x3_1); SBAR();
  MMA_V(acc[0][1][0], cat(u40_1_0, u40_1_1), cf[0][3]); const s16x4 u42_1_1 = ldtr(o.wb[1][1] + 10240); const unsigned x3_2_1 = cvt_asm(acc[1][0][1][2], acc[1][0][1][3]); SBAR();
  MMA_V(acc[0][1][1], cat(u40_1_0, u40_1_1), cf[1][3]); const s16x4 u42_2_0 = ldtr(o.wb[1][2] + 8192); const unsigned x3_2_2 = cvt_asm(acc[1][1][1][0], acc[1][1][1][1]); SBAR();
  MMA_V(acc[0][2][0], cat(u40_2_0, u40_2_1), cf[0][3]); const s16x4 u42_2_1 = ldtr(o.wb[1][2] + 10240); const unsigned x3_2_3 = cvt_asm(acc[1][1][1][2], acc[1][1][1][3]); SBAR();
  MMA_V(acc[0][2][1], cat(u40_2_0, u40_2_1), cf[1][3]); const s16x4 u42_3_0 = ldtr(o.wb[1][3] + 8192); const s16x4 u42_3_1 = ldtr(o.wb[1][3] + 10240); SBAR();
  MMA_V(acc[0][3][0], cat(u40_3_0, u40_3_1), cf[0][3]); const s16x4 u43_0_0 = ldtr(o.wb[1][0] + 12288); SBAR();
  MMA_V(acc[0][3][1], cat(u40_3_0, u40_3_1), cf[1][3]); const s16x4 u43_0_1 = ldtr(o.wb[1][0] + 14336); SBAR();
  MMA_V(acc[0][0][0], cat(u41_0_0, u41_0_1), cf[0][4]); const s16x4 u43_1_0 = ldtr(o.wb[1][1] + 12288); SBAR();
  MMA_V(acc[0][0][1], cat(u41_0_0, u41_0_1), cf[1][4]); const s16x4 u43_1_1 = ldtr(o.wb[1][1] + 14336); SBAR();
  LGKM(6); MMA_V(acc[0][1][0], cat(u41_1_0, u41_1_1), cf[0][4]); const s16x4 u43_2_0 = ldtr(o.wb[1][2] + 12288); SBAR();
  MMA_V(acc[0][1][1], cat(u41_1_0, u41_1_1), cf[1][4]); const s16x4 u43_2_1 = ldtr(o.wb[1][2] + 14336); SBAR();
  MMA_V(acc[0][2][0], cat(u41_2_0, u41_2_1), cf[0][4]); const s16x4 u43_3_0 = ldtr(o.wb[1][3] + 12288); SBAR();
  MMA_V(acc[0][2][1], cat(u41_2_0, u41_2_1), cf[1][4]); const s16x4 u43_3_1 = ldtr(o.wb[1][3] + 14336); SBAR();
  MMA_V(acc[0][3][0], cat(u41_3_0, u41_3_1), cf[0][4]); const s16x4 u44_0_0 = ldtr(o.wb[1][0] + 16384); SBAR();
  MMA_V(acc[0][3][1], cat(u41_3_0, u41_3_1), cf[1][4]); const s16x4 u44_0_1 = ldtr(o.wb[1][0] + 18432); const u32x4 x3_2 = {x3_2_0, x3_2_1, x3_2_2, x3_2_3}; st16_sc1(x3_2, c.voff + 2048, o.srdX, o.xo + 12288); SBAR();
  MMA_V(acc[0][0][0], cat(u42_0_0, u42_0_1), cf[0][5]); const s16x4 u44_1_0 = ldtr(o.wb[1][1] + 16384); const unsigned x3_3_0 = cvt_asm(acc[1][2][1][0], acc[1][2][1][1]); SBAR();
  MMA_V(acc[0][0][1], cat(u42_0_0, u42_0_1), cf[1][5]); const s16x4 u44_1_1 = ldtr(o.wb[1][1] + 18432); const unsigned x3_3_1 = cvt_asm(acc[1][2][1][2], acc[1][2][1][3]); KEEP(x3_2); SBAR();
  MMA_V(acc[0][1][0], cat(u42_1_0, u42_1_1), cf[0][5]); const s16x4 u44_2_0 = ldtr(o.wb[1][2] + 16384); const unsigned x3_3_2 = cvt_asm(acc[1][3][1][0], acc[1][3][1][1]); SBAR();
  MMA_V(acc[0][1][1], cat(u42_1_0, u42_1_1), cf[1][5]); const s16x4 u44_2_1 = ldtr(o.wb[1][2] + 18432); const unsigned x3_3_3 = cvt_asm(acc[1][3][1][2], acc[1][3][1][3]); SBAR();
  MMA_V(acc[0][2][0], cat(u42_2_0, u42_2_1), cf[0][5]); const s16x4 u44_3_0 = ldtr(o.wb[1][3] + 16384); SBAR();
  MMA_V(acc[0][2][1], cat(u42_2_0, u42_2_1), cf[1][5]); const s16x4 u44_3_1 = ldtr(o.wb[1][3] + 18432); SBAR();
  LGKM(5); MMA_V(acc[0][3][0], cat(u42_3_0, u42_3_1), cf[0][5]); const s16x4 u45_0_0 = ldtr(o.wb[1][0] + 20480); SBAR();
  MMA_V(acc[0][3][1], cat(u42_3_0, u42_3_1), cf[1][5]); const s16x4 u45_0_1 = ldtr(o.wb[1][0] + 22528); SBAR();
  MMA_V(acc[0][0][0], cat(u43_0_0, u43_0_1), cf[0][6]); const s16x4 u45_1_0 = ldtr(o.wb[1][1] + 20480); SBAR();
  MMA_V(acc[0][0][1], cat(u43_0_0, u43_0_1), cf[1][6]); const s16x4 u45_1_1 = ldtr(o.wb[1][1] + 22528); SBAR();
  MMA_V(acc[0][1][0], cat(u43_1_0, u43_1_1), cf[0][6]); const s16x4 u45_2_0 = ldtr(o.wb[1][2] + 20480); SBAR();
  MMA_V(acc[0][1][1], cat(u43_1_0, u43_1_1), cf[1][6]); const s16x4 u45_2_1 = ldtr(o.wb[1][2] + 22528); SBAR();
  MMA_V(acc[0][2][0], cat(u43_2_0, u43_2_1), cf[0][6]); const s16x4 u45_3_0 = ldtr(o.wb[1][3] + 20480); SBAR();
  MMA_V(acc[0][2][1], cat(u43_2_0, u43_2_1), cf[1][6]); const s16x4 u45_3_1 = ldtr(o.wb[1][3] + 22528); SBAR();
  MMA_V(acc[0][3][0], cat(u43_3_0, u43_3_1), cf[0][6]); const s16x4 u46_0_0 = ldtr(o.wb[1][0] + 24576); SBAR();
  MMA_V(acc[0][3][1], cat(u43_3_0, u43_3_1), cf[1][6]); const s16x4 u46_0_1 = ldtr(o.wb[1][0] + 26624); const u32x4 x3_3 = {x3_3_0, x3_3_1, x3_3_2, x3_3_3}; st16_sc1(x3_3, c.voff + 3072, o.srdX, o.xo + 12288); SBAR();
  MMA_V(acc[0][0][0], cat(u44_0_0, u44_0_1), cf[0][7]); const s16x4 u46_1_0 = ldtr(o.wb[1][1] + 24576); SBAR();
  MMA_V(acc[0][0][1], cat(u44_0_0, u44_0_1), cf[1][7]); const s16x4 u46_1_1 = ldtr(o.wb[1][1] + 26624); KEEP(x3_3); SBAR();
  LGKM(5); MMA_V(acc[0][1][0], cat(u44_1_0, u44_1_1), cf[0][7]); const s16x4 u46_2_0 = ldtr(o.wb[1][2] + 24576); SBAR();
  MMA_V(acc[0][1][1], cat(u44_1_0, u44_1_1), cf[1][7]); const s16x4 u46_2_1 = ldtr(o.wb[1][2] + 26624); SBAR();
  MMA_V(acc[0][2][0], cat(u44_2_0, u44_2_1), cf[0][7]); const s16x4 u46_3_0 = ldtr(o.wb[1][3] + 24576); SBAR();
  MMA_V(acc[0][2][1], cat(u44_2_0, u44_2_1), cf[1][7]); const s16x4 u46_3_1 = ldtr(o.wb[1][3] + 26624); SBAR();
  MMA_V(acc[0][3][0], cat(u44_3_0, u44_3_1), cf[0][7]); const s16x4 u47_0_0 = ldtr(o.wb[1][0] + 28672); SBAR();
  MMA_V(acc[0][3][1], cat(u44_3_0, u44_3_1), cf[1][7]); const s16x4 u47_0_1 = ldtr(o.wb[1][0] + 30720); SBAR();
  MMA_V0(acc[1][0][0], cat(u45_0_0, u45_0_1), cf[0][8]); const s16x4 u47_1_0 = ldtr(o.wb[1][1] + 28672); SBAR();
  MMA_V0(acc[1][0][1], cat(u45_0_0, u45_0_1), cf[1][8]); const s16x4 u47_1_1 = ldtr(o.wb[1][1] + 30720); SBAR();
  MMA_V0(acc[1][1][0], cat(u45_1_0, u45_1_1), cf[0][8]); const s16x4 u47_2_0 = ldtr(o.wb[1][2] + 28672); SBAR();
  MMA_V0(acc[1][1][1], cat(u45_1_0, u45_1_1), cf[1][8]); const s16x4 u47_2_1 = ldtr(o.wb[1][2] + 30720); SBAR();
  MMA_V0(acc[1][2][0], cat(u45_2_0, u45_2_1), cf[0][8]); const s16x4 u47_3_0 = ldtr(o.wb[1][3] + 28672); SBAR();
  MMA_V0(acc[1][2][1], cat(u45_2_0, u45_2_1), cf[1][8]); const s16x4 u47_3_1 = ldtr(o.wb[1][3] + 30720); SBAR();
  LGKM(5); MMA_V0(acc[1][3][0], cat(u45_3_0, u45_3_1), cf[0][8]); const unsigned x4_0_0 = cvt_asm(acc[0][0][0][0], acc[0][0][0][1]); const unsigned x4_0_1 = cvt_asm(acc[0][0][0][2], acc[0][0][0][3]); SBAR();
  MMA_V0(acc[1][3][1], cat(u45_3_0, u45_3_1), cf[1][8]); const unsigned x4_0_2 = cvt_asm(acc[0][1][0][0], acc[0][1][0][1]); const unsigned x4_0_3 = cvt_asm(acc[0][1][0][2], acc[0][1][0][3]); SBAR();
  MMA_V(acc[1][0][0], cat(u46_0_0, u46_0_1), cf[0][0]); SBAR();
  MMA_V(acc[1][0][1], cat(u46_0_0, u46_0_1), cf[1][0]); SBAR();
  MMA_V(acc[1][1][0], cat(u46_1_0, u46_1_1), cf[0][0]); LGKM(0); asm volatile("s_waitcnt vmcnt(19)\n\ts_barrier" ::: ACLOB, "memory"); SBAR();
  MMA_V(acc[1][1][1], cat(u46_1_0, u46_1_1), cf[1][0]); const s16x4 u48_0_0 = ldtr(o.wb[1][0] + 32768); SBAR();
  MMA_V(acc[1][2][0], cat(u46_2_0, u46_2_1), cf[0][0]); const s16x4 u48_0_1 = ldtr(o.wb[1][0] + 34816); SBAR();
  MMA_V(acc[1][2][1], cat(u46_2_0, u46_2_1), cf[1][0]); const s16x4 u48_1_0 = ldtr(o.wb[1][1] + 32768); const s16x4 u48_1_1 = ldtr(o.wb[1][1] + 34816); SBAR();
  MMA_V(acc[1][3][0], cat(u46_3_0, u46_3_1), cf[0][0]); const s16x4 u48_2_0 = ldtr(o.wb[1][2] + 32768); SBAR();
  MMA_V(acc[1][3][1], cat(u46_3_0, u46_3_1), cf[1][0]); const s16x4 u48_2_1 = ldtr(o.wb[1][2] + 34816); SBAR();
  MMA_V(acc[1][0][0], cat(u47_0_0, u47_0_1), cf[0][1]); const s16x4 u48_3_0 = ldtr(o.wb[1][3] + 32768); SBAR();
  MMA_V(acc[1][0][1], cat(u47_0_0, u47_0_1), cf[1][1]); const s16x4 u48_3_1 = ldtr(o.wb[1][3] + 34816); const s16x4 u49_0_0 = ldtr(o.wb[1][0] + 36864); SBAR();
  MMA_V(acc[1][1][0], cat(u47_1_0, u47_1_1), cf[0][1]); const s16x4 u49_0_1 = ldtr(o.wb[1][0] + 38912); SBAR();
  MMA_V(acc[1][1][1], cat(u47_1_0, u47_1_1), cf[1][1]); const s16x4 u49_1_0 = ldtr(o.wb[1][1] + 36864); SBAR();
  MMA_V(acc[1][2][0], cat(u47_2_0, u47_2_1), cf[0][1]); const s16x4 u49_1_1 = ldtr(o.wb[1][1] + 38912); SBAR();
  MMA_V(acc[1][2][1], cat(u47_2_0, u47_2_1), cf[1][1]); const s16x4 u49_2_0 = ldtr(o.wb[1][2] + 36864); const s16x4 u49_2_1 = ldtr(o.wb[1][2] + 38912); SBAR();
  MMA_V(acc[1][3][0], cat(u47_3_0, u47_3_1), cf[0][1]); const s16x4 u49_3_0 = ldtr(o.wb[1][3] + 36864); SBAR();
  MMA_V(acc[1][3][1], cat(u47_3_0, u47_3_1), cf[1][1]); const s16x4 u49_3_1 = ldtr(o.wb[1][3] + 38912); const u32x4 x4_0 = {x4_0_0, x4_0_1, x4_0_2, x4_0_3}; st16_sc1(x4_0, c.voff + 0, o.srdX, o.xo + 16384); SBAR();
  LGKM(6); MMA_V(acc[1][0][0], cat(u48_0_0, u48_0_1), cf[0][2]); const s16x4 u50_0_0 = ldtr(o.wb[1][0] + 40960); const unsigned x4_1_0 = cvt_asm(acc[0][2][0][0], acc[0][2][0][1]); SBAR();
  MMA_V(acc[1][0][1], cat(u48_0_0, u48_0_1), cf[1][2]); const s16x4 u50_0_1 = ldtr(o.wb[1][0] + 43008); const s16x4 u50_1_0 = ldtr(o.wb[1][1] + 40960); KEEP(x4_0); SBAR();
  MMA_V(acc[1][1][0], cat(u48_1_0, u48_1_1), cf[0][2]); const s16x4 u50_1_1 = ldtr(o.wb[1][1] + 43008); const unsigned x4_1_1 = cvt_asm(acc[0][2][0][2], acc[0][2][0][3]); SBAR();
  MMA_V(acc[1][1][1], cat(u48_1_0, u48_1_1), cf[1][2]); const s16x4 u50_2_0 = ldtr(o.wb[1][2] + 40960); const unsigned x4_1_2 = cvt_asm(acc[0][3][0][0], acc[0][3][0][1]); SBAR();
  MMA_V(acc[1][2][0], cat(u48_2_0, u48_2_1), cf[0][2]); const s16x4 u50_2_1 = ldtr(o.wb[1][2] + 43008); const unsigned x4_1_3 = cvt_asm(acc[0][3][0][2], acc[0][3][0][3]); SBAR();
  MMA_V(acc[1][2][1], cat(u48_2_0, u48_2_1), cf[1][2]); const s16x4 u50_3_0 = ldtr(o.wb[1][3] + 40960); const s16x4 u50_3_1 = ldtr(o.wb[1][3] + 43008); SBAR();
  MMA_V(acc[1][3][0], cat(u48_3_0, u48_3_1), cf[0][2]); const s16x4 u51_0_0 = ldtr(o.wb[1][0] + 45056); SBAR();
  MMA_V(acc[1][3][1], cat(u48_3_0, u48_3_1), cf[1][2]); const s16x4 u51_0_1 = ldtr(o.wb[1][0] + 47104); SBAR();
  MMA_V(acc[1][0][0], cat(u49_0_0, u49_0_1), cf[0][3]); const s16x4 u51_1_0 = ldtr(o.wb[1][1] + 45056); SBAR();
  MMA_V(acc[1][0][1], cat(u49_0_0, u49_0_1), cf[1][3]); const s16x4 u51_1_1 = ldtr(o.wb[1][1] + 47104); SBAR();
  LGKM(6); MMA_V(acc[1][1][0], cat(u49_1_0, u49_1_1), cf[0][3]); const s16x4 u51_2_0 = ldtr(o.wb[1][2] + 45056); SBAR();
  MMA_V(acc[1][1][1], cat(u49_1_0, u49_1_1), cf[1][3]); const s16x4 u51_2_1 = ldtr(o.wb[1][2] + 47104); SBAR();
  MMA_V(acc[1][2][0], cat(u49_2_0, u49_2_1), cf[0][3]); const s16x4 u51_3_0 = ldtr(o.wb[1][3] + 45056); SBAR();
  MMA_V(acc[1][2][1], cat(u49_2_0, u49_2_1), cf[1][3]); const s16x4 u51_3_1 = ldtr(o.wb[1][3] + 47104); SBAR();
  MMA_V(acc[1][3][0], cat(u49_3_0, u49_3_1), cf[0][3]); const s16x4 u52_0_0 = ldtr(o.wb[1][0] + 49152); SBAR();
  MMA_V(acc[1][3][1], cat(u49_3_0, u49_3_1), cf[1][3]); const s16x4 u52_0_1 = ldtr(o.wb[1][0] + 51200); const u32x4 x4_1 = {x4_1_0, x4_1_1, x4_1_2, x4_1_3}; st16_sc1(x4_1, c.voff + 1024, o.srdX, o.xo + 16384); SBAR();
  MMA_V(acc[1][0][0], cat(u50_0_0, u50_0_1), cf[0][4]); const s16x4 u52_1_0 = ldtr(o.wb[1][1] + 49152); const unsigned x4_2_0 = cvt_asm(acc[0][0][1][0], acc[0][0][1][1]); asm volatile("s_waitcnt vmcnt(2)" ::: ACLOB, "memory"); O_PUBLISH(0); SBAR();
  MMA_V(acc[1][0][1], cat(u50_0_0, u50_0_1), cf[1][4]); const s16x4 u52_1_1 = ldtr(o.wb[1][1] + 51200); const unsigned x4_2_1 = cvt_asm(acc[0][0][1][2], acc[0][0][1][3]); KEEP(x4_1); SBAR();
  MMA_V(acc[1][1][0], cat(u50_1_0, u50_1_1), cf[0][4]); const s16x4 u52_2_0 = ldtr(o.wb[1][2] + 49152); const unsigned x4_2_2 = cvt_asm(acc[0][1][1][0], acc[0][1][1][1]); SBAR();
  MMA_V(acc[1][1][1], cat(u50_1_0, u50_1_1), cf[1][4]); const s16x4 u52_2_1 = ldtr(o.wb[1][2] + 51200); const unsigned x4_2_3 = cvt_asm(acc[0][1][1][2], acc[0][1][1][3]); SBAR();
  MMA_V(acc[1][2][0], cat(u50_2_0, u50_2_1), cf[0][4]); const s16x4 u52_3_0 = ldtr(o.wb[1][3] + 49152); SBAR();
  MMA_V(acc[1][2][1], cat(u50_2_0, u50_2_1), cf[1][4]); const s16x4 u52_3_1 = ldtr(o.wb[1][3] + 51200); SBAR();
  LGKM(5); MMA_V(acc[1][3][0], cat(u50_3_0, u50_3_1), cf[0][4]); const s16x4 u53_0_0 = ldtr(o.wb[1][0] + 53248); SBAR();
  MMA_V(acc[1][3][1], cat(u50_3_0, u50_3_1), cf[1][4]); const s16x4 u53_0_1 = ldtr(o.wb[1][0] + 55296); SBAR();
  MMA_V(acc[1][0][0], cat(u51_0_0, u51_0_1), cf[0][5]); const s16x4 u53_1_0 = ldtr(o.wb[1][1] + 53248); SBAR();
  MMA_V(acc[1][0][1], cat(u51_0_0, u51_0_1), cf[1][5]); const s16x4 u53_1_1 = ldtr(o.wb[1][1] + 55296); SBAR();
  MMA_V(acc[1][1][0], cat(u51_1_0, u51_1_1), cf[0][5]); const s16x4 u53_2_0 = ldtr(o.wb[1][2] + 53248); SBAR();
  MMA_V(acc[1][1][1], cat(u51_1_0, u51_1_1), cf[1][5]); const s16x4 u53_2_1 = ldtr(o.wb[1][2] + 55296); SBAR();
  MMA_V(acc[1][2][0], cat(u51_2_0, u51_2_1), cf[0][5]); const s16x4 u53_3_0 = ldtr(o.wb[1][3] + 53248); SBAR();
  MMA_V(acc[1][2][1], cat(u51_2_0, u51_2_1), cf[1][5]); const s16x4 u53_3_1 = ldtr(o.wb[1][3] + 55296); SBAR();
  MMA_V(acc[1][3][0], cat(u51_3_0, u51_3_1), cf[0][5]); const s16x4 u54_0_0 = ldtr(o.wb[1][0] + 57344); SBAR();
  MMA_V(acc[1][3][1], cat(u51_3_0, u51_3_1), cf[1][5]); const s16x4 u54_0_1 = ldtr(o.wb[1][0] + 59392); const u32x4 x4_2 = {x4_2_0, x4_2_1, x4_2_2, x4_2_3}; st16_sc1(x4_2, c.voff + 2048, o.srdX, o.xo + 16384); SBAR();
  MMA_V(acc[1][0][0], cat(u52_0_0, u52_0_1), cf[0][6]); const s16x4 u54_1_0 = ldtr(o.wb[1][1] + 57344); const unsigned x4_3_0 = cvt_asm(acc[0][2][1][0], acc[0][2][1][1]); SBAR();
  MMA_V(acc[1][0][1], cat(u52_0_0, u52_0_1), cf[1][6]); const s16x4 u54_1_1 = ldtr(o.wb[1][1] + 59392); const unsigned x4_3_1 = cvt_asm(acc[0][2][1][2], acc[0][2][1][3]); KEEP(x4_2); SBAR();
  LGKM(5); MMA_V(acc[1][1][0], cat(u52_1_0, u52_1_1), cf[0][6]); const s16x4 u54_2_0 = ldtr(o.wb[1][2] + 57344); const unsigned x4_3_2 = cvt_asm(acc[0][3][1][0], acc[0][3][1][1]); SBAR();
  MMA_V(acc[1][1][1], cat(u52_1_0, u52_1_1), cf[1][6]); const s16x4 u54_2_1 = ldtr(o.wb[1][2] + 59392); const unsigned x4_3_3 = cvt_asm(acc[0][3][1][2], acc[0][3][1][3]); SBAR();
  MMA_V(acc[1][2][0], cat(u52_2_0, u52_2_1), cf[0][6]); const s16x4 u54_3_0 = ldtr(o.wb[1][3] + 57344); SBAR();
  MMA_V(acc[1][2][1], cat(u52_2_0, u52_2_1), cf[1][6]); const s16x4 u54_3_1 = ldtr(o.wb[1][3] + 59392); SBAR();
  MMA_V(acc[1][3][0], cat(u52_3_0, u52_3_1), cf[0][6]); const s16x4 u55_0_0 = ldtr(o.wb[1][0] + 61440); SBAR();
  MMA_V(acc[1][3][1], cat(u52_3_0, u52_3_1), cf[1][6]); const s16x4 u55_0_1 = ldtr(o.wb[1][0] + 63488); SBAR();
  MMA_V(acc[1][0][0], cat(u53_0_0, u53_0_1), cf[0][7]); const s16x4 u55_1_0 = ldtr(o.wb[1][1] + 61440); SBAR();
  MMA_V(acc[1][0][1], cat(u53_0_0, u53_0_1), cf[1][7]); const s16x4 u55_1_1 = ldtr(o.wb[1][1] + 63488); SBAR();
  MMA_V(acc[1][1][0], cat(u53_1_0, u53_1_1), cf[0][7]); const s16x4 u55_2_0 = ldtr(o.wb[1][2] + 61440); SBAR();
  MMA_V(acc[1][1][1], cat(u53_1_0, u53_1_1), cf[1][7]); const s16x4 u55_2_1 = ldtr(o.wb[1][2] + 63488); SBAR();
  MMA_V(acc[1][2][0], cat(u53_2_0, u53_2_1), cf[0][7]); const s16x4 u55_3_0 = ldtr(o.wb[1][3] + 61440); SBAR();
  MMA_V(acc[1][2][1], cat(u53_2_0, u53_2_1), cf[1][7]); const s16x4 u55_3_1 = ldtr(o.wb[1][3] + 63488); SBAR();
  LGKM(5); MMA_V(acc[1][3][0], cat(u53_3_0, u53_3_1), cf[0][7]); SBAR();
  MMA_V(acc[1][3][1], cat(u53_3_0, u53_3_1), cf[1][7]); const u32x4 x4_3 = {x4_3_0, x4_3_1, x4_3_2, x4_3_3}; st16_sc1(x4_3, c.voff + 3072, o.srdX, o.xo + 16384); SBAR();
  MMA_V0(acc[0][0][0], cat(u54_0_0, u54_0_1), cf[0][8]); SBAR();
  MMA_V0(acc[0][0][1], cat(u54_0_0, u54_0_1), cf[1][8]); KEEP(x4_3); SBAR();
  MMA_V0(acc[0][1][0], cat(u54_1_0, u54_1_1), cf[0][8]); LGKM(0); asm volatile("s_waitcnt vmcnt(13)\n\ts_barrier" ::: ACLOB, "memory"); SBAR();
  MMA_V0(acc[0][1][1], cat(u54_1_0, u54_1_1), cf[1][8]); const s16x4 u56_0_0 = ldtr(o.wb[2][0] + 0); SBAR();
  MMA_V0(acc[0][2][0], cat(u54_2_0, u54_2_1), cf[0][8]); const s16x4 u56_0_1 = ldtr(o.wb[2][0] + 2048); SBAR();
  MMA_V0(acc[0][2][1], cat(u54_2_0, u54_2_1), cf[1][8]); const s16x4 u56_1_0 = ldtr(o.wb[2][1] + 0); const s16x4 u56_1_1 = ldtr(o.wb[2][1] + 2048); SBAR();
  MMA_V0(acc[0][3][0], cat(u54_3_0, u54_3_1), cf[0][8]); const s16x4 u56_2_0 = ldtr(o.wb[2][2] + 0); const unsigned x5_0_0 = cvt_asm(acc[1][0][0][0], acc[1][0][0][1]); SBAR();
  MMA_V0(acc[0][3][1], cat(u54_3_0, u54_3_1), cf[1][8]); const s16x4 u56_2_1 = ldtr(o.wb[2][2] + 2048); const unsigned x5_0_1 = cvt_asm(acc[1][0][0][2], acc[1][0][0][3]); SBAR();
  MMA_V(acc[0][0][0], cat(u55_0_0, u55_0_1), cf[0][0]); const s16x4 u56_3_0 = ldtr(o.wb[2][3] + 0); const unsigned x5_0_2 = cvt_asm(acc[1][1][0][0], acc[1][1][0][1]); SBAR();
  MMA_V(acc[0][0][1], cat(u55_0_0, u55_0_1), cf[1][0]); const s16x4 u56_3_1 = ldtr(o.wb[2][3] + 2048); const s16x4 u57_0_0 = ldtr(o.wb[2][0] + 4096); SBAR();
  MMA_V(acc[0][1][0], cat(u55_1_0, u55_1_1), cf[0][0]); const s16x4 u57_0_1 = ldtr(o.wb[2][0] + 6144); const unsigned x5_0_3 = cvt_asm(acc[1][1][0][2], acc[1][1][0][3]); SBAR();
  MMA_V(acc[0][1][1], cat(u55_1_0, u55_1_1), cf[1][0]); const s16x4 u57_1_0 = ldtr(o.wb[2][1] + 4096); SBAR();
  MMA_V(acc[0][2][0], cat(u55_2_0, u55_2_1), cf[0][0]); const s16x4 u57_1_1 = ldtr(o.wb[2][1] + 6144); SBAR();
  MMA_V(acc[0][2][1], cat(u55_2_0, u55_2_1), cf[1][0]); const s16x4 u57_2_0 = ldtr(o.wb[2][2] + 4096); const s16x4 u57_2_1 = ldtr(o.wb[2][2] + 6144); SBAR();
  MMA_V(acc[0][3][0], cat(u55_3_0, u55_3_1), cf[0][0]); const s16x4 u57_3_0 = ldtr(o.wb[2][3] + 4096); SBAR();
  MMA_V(acc[0][3][1], cat(u55_3_0, u55_3_1), cf[1][0]); const s16x4 u57_3_1 = ldtr(o.wb[2][3] + 6144); const u32x4 x5_0 = {x5_0_0, x5_0_1, x5_0_2, x5_0_3}; st16_sc1(x5_0, c.voff + 0, o.srdX, o.xo + 20480); SBAR();
  LGKM(6); MMA_V(acc[0][0][0], cat(u56_0_0, u56_0_1), cf[0][1]); const s16x4 u58_0_0 = ldtr(o.wb[2][0] + 8192); const unsigned x5_1_0 = cvt_asm(acc[1][2][0][0], acc[1][2][0][1]); SBAR();
  MMA_V(acc[0][0][1], cat(u56_0_0, u56_0_1), cf[1][1]); const s16x4 u58_0_1 = ldtr(o.wb[2][0] + 10240); const s16x4 u58_1_0 = ldtr(o.wb[2][1] + 8192); KEEP(x5_0); SBAR();
  MMA_V(acc[0][1][0], cat(u56_1_0, u56_1_1), cf[0][1]); const s16x4 u58_1_1 = ldtr(o.wb[2][1] + 10240); const unsigned x5_1_1 = cvt_asm(acc[1][2][0][2], acc[1][2][0][3]); SBAR();
  MMA_V(acc[0][1][1], cat(u56_1_0, u56_1_1), cf[1][1]); const s16x4 u58_2_0 = ldtr(o.wb[2][2] + 8192); const unsigned x5_1_2 = cvt_asm(acc[1][3][0][0], acc[1][3][0][1]); SBAR();
  MMA_V(acc[0][2][0], cat(u56_2_0, u56_2_1), cf[0][1]); const s16x4 u58_2_1 = ldtr(o.wb[2][2] + 10240); const unsigned x5_1_3 = cvt_asm(acc[1][3][0][2], acc[1][3][0][3]); SBAR();
  MMA_V(acc[0][2][1], cat(u56_2_0, u56_2_1), cf[1][1]); const s16x4 u58_3_0 = ldtr(o.wb[2][3] + 8192); const s16x4 u58_3_1 = ldtr(o.wb[2][3] + 10240); SBAR();
  MMA_V(acc[0][3][0], cat(u56_3_0, u56_3_1), cf[0][1]); const s16x4 u59_0_0 = ldtr(o.wb[2][0] + 12288); SBAR();
  MMA_V(acc[0][3][1], cat(u56_3_0, u56_3_1), cf[1][1]); const s16x4 u59_0_1 = ldtr(o.wb[2][0] + 14336); const u32x4 x5_1 = {x5_1_0, x5_1_1, x5_1_2, x5_1_3}; st16_sc1(x5_1, c.voff + 1024, o.srdX, o.xo + 20480); SBAR();
  MMA_V(acc[0][0][0], cat(u57_0_0, u57_0_1), cf[0][2]); const s16x4 u59_1_0 = ldtr(o.wb[2][1] + 12288); const unsigned x5_2_0 = cvt_asm(acc[1][0][1][0], acc[1][0][1][1]); SBAR();
  MMA_V(acc[0][0][1], cat(u57_0_0, u57_0_1), cf[1][2]); const s16x4 u59_1_1 = ldtr(o.wb[2][1] + 14336); const unsigned x5_2_1 = cvt_asm(acc[1][0][1][2], acc[1][0][1][3]); KEEP(x5_1); SBAR();
  LGKM(6); MMA_V(acc[0][1][0], cat(u57_1_0, u57_1_1), cf[0][2]); const s16x4 u59_2_0 = ldtr(o.wb[2][2] + 12288); const unsigned x5_2_2 = cvt_asm(acc[1][1][1][0], acc[1][1][1][1]); SBAR();
  MMA_V(acc[0][1][1], cat(u57_1_0, u57_1_1), cf[1][2]); const s16x4 u59_2_1 = ldtr(o.wb[2][2] + 14336); const unsigned x5_2_3 = cvt_asm(acc[1][1][1][2], acc[1][1][1][3]); SBAR();
  MMA_V(acc[0][2][0], cat(u57_2_0, u57_2_1), cf[0][2]); const s16x4 u59_3_0 = ldtr(o.wb[2][3] + 12288); SBAR();
  MMA_V(acc[0][2][1], cat(u57_2_0, u57_2_1), cf[1][2]); const s16x4 u59_3_1 = ldtr(o.wb[2][3] + 14336); SBAR();
  MMA_V(acc[0][3][0], cat(u57_3_0, u57_3_1), cf[0][2]); const s16x4 u60_0_0 = ldtr(o.wb[2][0] + 16384); SBAR();
  MMA_V(acc[0][3][1], cat(u57_3_0, u57_3_1), cf[1][2]); const s16x4 u60_0_1 = ldtr(o.wb[2][0] + 18432); const u32x4 x5_2 = {x5_2_0, x5_2_1, x5_2_2, x5_2_3}; st16_sc1(x5_2, c.voff + 2048, o.srdX, o.xo + 20480); SBAR();
  MMA_V(acc[0][0][0], cat(u58_0_0, u58_0_1), cf[0][3]); const s16x4 u60_1_0 = ldtr(o.wb[2][1] + 16384); const unsigned x5_3_0 = cvt_asm(acc[1][2][1][0], acc[1][2][1][1]); SBAR();
  MMA_V(acc[0][0][1], cat(u58_0_0, u58_0_1), cf[1][3]); const s16x4 u60_1_1 = ldtr(o.wb[2][1] + 18432); const unsigned x5_3_1 = cvt_asm(acc[1][2][1][2], acc[1][2][1][3]); KEEP(x5_2); SBAR();
  MMA_V(acc[0][1][0], cat(u58_1_0, u58_1_1), cf[0][3]); const s16x4 u60_2_0 = ldtr(o.wb[2][2] + 16384); const unsigned x5_3_2 = cvt_asm(acc[1][3][1][0], acc[1][3][1][1]); SBAR();
  MMA_V(acc[0][1][1], cat(u58_1_0, u58_1_1), cf[1][3]); const s16x4 u60_2_1 = ldtr(o.wb[2][2] + 18432); const unsigned x5_3_3 = cvt_asm(acc[1][3][1][2], acc[1][3][1][3]); SBAR();
  MMA_V(acc[0][2][0], cat(u58_2_0, u58_2_1), cf[0][3]); const s16x4 u60_3_0 = ldtr(o.wb[2][3] + 16384); SBAR();
  MMA_V(acc[0][2][1], cat(u58_2_0, u58_2_1), cf[1][3]); const s16x4 u60_3_1 = ldtr(o.wb[2][3] + 18432); SBAR();
  LGKM(5); MMA_V(acc[0][3][0], cat(u58_3_0, u58_3_1), cf[0][3]); const s16x4 u61_0_0 = ldtr(o.wb[2][0] + 20480); SBAR();
  MMA_V(acc[0][3][1], cat(u58_3_0, u58_3_1), cf[1][3]); const s16x4 u61_0_1 = ldtr(o.wb[2][0] + 22528); const u32x4 x5_3 = {x5_3_0, x5_3_1, x5_3_2, x5_3_3}; st16_sc1(x5_3, c.voff + 3072, o.srdX, o.xo + 20480); SBAR();
  MMA_V(acc[0][0][0], cat(u59_0_0, u59_0_1), cf[0][4]); const s16x4 u61_1_0 = ldtr(o.wb[2][1] + 20480); SBAR();
  MMA_V(acc[0][0][1], cat(u59_0_0, u59_0_1), cf[1][4]); const s16x4 u61_1_1 = ldtr(o.wb[2][1] + 22528); KEEP(x5_3); SBAR();
  MMA_V(acc[0][1][0], cat(u59_1_0, u59_1_1), cf[0][4]); const s16x4 u61_2_0 = ldtr(o.wb[2][2] + 20480); SBAR();
  MMA_V(acc[0][1][1], cat(u59_1_0, u59_1_1), cf[1][4]); const s16x4 u61_2_1 = ldtr(o.wb[2][2] + 22528); SBAR();
  MMA_V(acc[0][2][0], cat(u59_2_0, u59_2_1), cf[0][4]); const s16x4 u61_3_0 = ldtr(o.wb[2][3] + 20480); SBAR();
  MMA_V(acc[0][2][1], cat(u59_2_0, u59_2_1), cf[1][4]); const s16x4 u61_3_1 = ldtr(o.wb[2][3] + 22528); SBAR();
  MMA_V(acc[0][3][0], cat(u59_3_0, u59_3_1), cf[0][4]); const s16x4 u62_0_0 = ldtr(o.wb[2][0] + 24576); SBAR();
  MMA_V(acc[0][3][1], cat(u59_3_0, u59_3_1), cf[1][4]); const s16x4 u62_0_1 = ldtr(o.wb[2][0] + 26624); SBAR();
  MMA_V(acc[0][0][0], cat(u60_0_0, u60_0_1), cf[0][5]); const s16x4 u62_1_0 = ldtr(o.wb[2][1] + 24576); O_POLL_ISSUE(pending); SBAR();
  MMA_V(acc[0][0][1], cat(u60_0_0, u60_0_1), cf[1][5]); const s16x4 u62_1_1 = ldtr(o.wb[2][1] + 26624); SBAR();
  LGKM(5); MMA_V(acc[0][1][0], cat(u60_1_0, u60_1_1), cf[0][5]); const s16x4 u62_2_0 = ldtr(o.wb[2][2] + 24576); SBAR();
  MMA_V(acc[0][1][1], cat(u60_1_0, u60_1_1), cf[1][5]); const s16x4 u62_2_1 = ldtr(o.wb[2][2] + 26624); SBAR();
  MMA_V(acc[0][2][0], cat(u60_2_0, u60_2_1), cf[0][5]); const s16x4 u62_3_0 = ldtr(o.wb[2][3] + 24576); SBAR();
  MMA_V(acc[0][2][1], cat(u60_2_0, u60_2_1), cf[1][5]); const s16x4 u62_3_1 = ldtr(o.wb[2][3] + 26624); SBAR();
  MMA_V(acc[0][3][0], cat(u60_3_0, u60_3_1), cf[0][5]); SBAR();
  MMA_V(acc[0][3][1], cat(u60_3_0, u60_3_1), cf[1][5]); SBAR();
  MMA_V(acc[0][0][0], cat(u61_0_0, u61_0_1), cf[0][6]); SBAR();
  MMA_V(acc[0][0][1], cat(u61_0_0, u61_0_1), cf[1][6]); SBAR();
  MMA_V(acc[0][1][0], cat(u61_1_0, u61_1_1), cf[0][6]); SBAR();
  MMA_V(acc[0][1][1], cat(u61_1_0, u61_1_1), cf[1][6]); SBAR();
  MMA_V(acc[0][2][0], cat(u61_2_0, u61_2_1), cf[0][6]); SBAR();
  MMA_V(acc[0][2][1], cat(u61_2_0, u61_2_1), cf[1][6]); SBAR();
  LGKM(0); MMA_V(acc[0][3][0], cat(u61_3_0, u61_3_1), cf[0][6]); SBAR();
  MMA_V(acc[0][3][1], cat(u61_3_0, u61_3_1), cf[1][6]); SBAR();
  MMA_V(acc[0][0][0], cat(u62_0_0, u62_0_1), cf[0][7]); SBAR();
  MMA_V(acc[0][0][1], cat(u62_0_0, u62_0_1), cf[1][7]); SBAR();
  MMA_V(acc[0][1][0], cat(u62_1_0, u62_1_1), cf[0][7]); SBAR();
  MMA_V(acc[0][1][1], cat(u62_1_0, u62_1_1), cf[1][7]); SBAR();
  MMA_V(acc[0][2][0], cat(u62_2_0, u62_2_1), cf[0][7]); SBAR();
  MMA_V(acc[0][2][1], cat(u62_2_0, u62_2_1), cf[1][7]); SBAR();
  MMA_V(acc[0][3][0], cat(u62_3_0, u62_3_1), cf[0][7]); SBAR();
  MMA_V(acc[0][3][1], cat(u62_3_0, u62_3_1), cf[1][7]); SBAR();
  asm volatile("s_nop 7" ::: "memory"); O_LAST(0);

#pragma unroll
  for (int u = 0; u < 4; ++u) { PIN_V(acc[0][2 * (u & 1)][u >> 1]); PIN_V(acc[0][2 * (u & 1) + 1][u >> 1]); st16_pad(pack8(acc[0][2 * (u & 1)][u >> 1], acc[0][2 * (u & 1) + 1][u >> 1]), c.voff + u * 1024, o.srdX, o.xo + 6 * 4096); }
  asm volatile("s_waitcnt vmcnt(5)" ::: "memory");
  O_PUBLISH(1);
  asm volatile("s_waitcnt vmcnt(1)" : "+v"(fl5) :: "memory");
  O_PUBLISH(2);
  bf16x8 sb = {se[0], se[1], se[0], se[1]}; PIN_V(sb);
#pragma nounroll
  for (unsigned spin = 0; spin < (1u << 16); ++spin) {
    const unsigned nw = (unsigned)__builtin_amdgcn_ballot_w64(flag_ok(fl5, w.tkR)) & pending;
    pending &= ~nw;
    if (nw != 0u) gather_all(o.srdX, c.voff, o.g0, nw, make_iseq<32>{}); else __builtin_amdgcn_s_sleep(POLLSLEEP);
    if (pending == 0u) break;
    O_POLL_ISSUE(pending);
    asm volatile("s_waitcnt vmcnt(0)" : "+v"(fl5) :: "memory");
  }
  ctr_bump1(w.ws + WS_SYNC + SY_CTRH + w.head * 256, w.wid | w.j);
  ctr_bump1(w.ws + WS_SYNC + SY_CTRR + w.rb * 256, (w.wid ^ 1u) | w.head);
  asm volatile("s_waitcnt vmcnt(0)" ::: ACLOB, "memory");
  f32x4 d0, d1, d2, d3;
  sum_unit<0>(d0, d1, se, so, sb, make_iseq<8>{});
  sum_unit<64>(d2, d3, se, so, sb, make_iseq<8>{});
  PIN_V(d0); PIN_V(d1);
  out_unit(d0, d1, o.u0, lane, w, out);
  asm volatile("s_nop 7" : "+v"(d2), "+v"(d3));
  if (o.u0 < 12) out_unit(d2, d3, 16 + o.u0, lane, w, out);
}

__global__ __launch_bounds__(256, 1) void fused_block(const u16* x, const u16* ckv, const u16* wq, const u16* wuk, const u16* wuv, const u16* wo, const u16* wqkv, const u16* bwo, const u16* gw, char* ws, u16* out) {
  extern __shared__ __attribute__((aligned(16))) char smem[];
  Wg w;
  { u64 pc; unsigned t;
    asm volatile("s_getpc_b64 %0\n\tv_lshlrev_b32 %2, 4, %3\n\tglobal_load_dword %1, %2, %0" : "=&s"(pc), "=&v"(w.warm), "=&v"(t) : "v"(threadIdx.x) : "memory");
    SBAR();
    w.code = (const char*)(pc & ~255ull); }
  w.ws = ws; w.lds = (const LAS char*)smem; w.lds0 = (unsigned)(uintptr_t)w.lds;
  w.lane = threadIdx.x & 63; w.wid = rfl(threadIdx.x >> 6);
  const int b = blockIdx.x, idx = b >> 3;
  w.head = (b & 7) * 2 + (idx & 1); w.j = idx >> 1; w.p = w.j & 7; w.roleA = w.j >= 8; w.rb = w.roleA ? 15 - w.p : w.p;
  const In in = {x, ckv, wq, wuk, wuv, wo, wqkv, bwo, gw, out};
  phase1(w, in);
  phase23(w, out);
}
extern "C" void kernel_launch(void* const* d_in, const int* in_sizes, int n_in, void* d_out, int out_size, void* d_ws, size_t ws_size, hipStream_t stream) {
  (void)hipFuncSetAttribute((const void*)fused_block, hipFuncAttributeMaxDynamicSharedMemorySize, 163840);
  fused_block<<<256, 256, 163840, stream>>>((const u16*)d_in[0], (const u16*)d_in[1], (const u16*)d_in[2], (const u16*)d_in[3], (const u16*)d_in[4], (const u16*)d_in[5],
                                            (const u16*)d_in[7], (const u16*)d_in[8], (const u16*)d_in[9], (char*)d_ws, (u16*)d_out);
}
```
